```python
import torch

def _softmax(x, axis=-1):
    return torch.softmax(x, dim=axis)
def _gelu(x):
    return torch.nn.functional.gelu(x, approximate="tanh")
def _silu(x):
    return torch.nn.functional.silu(x)
def _sigmoid(x):
    return torch.sigmoid(x)
def _layernorm(x, axis=-1, eps=1e-5):
    m = x.mean(dim=axis, keepdim=True)
    v = x.var(dim=axis, keepdim=True, unbiased=False)
    return (x - m) / torch.sqrt(v + eps)
def _scatter_add(tgt, idx, src, axis=0):
    return tgt.clone().index_add_(axis, idx, src)
def _segment_sum(src, seg, n):
    out = torch.zeros((n,) + src.shape[1:], dtype=src.dtype)
    return out.index_add_(0, seg, src)
def _pad(x, pw):
    flat = tuple(v for pair in reversed(pw) for v in pair)
    return torch.nn.functional.pad(x, flat)

kernel_name = 'parallel_sum__sliding_block__swiglu_ffn__prompt'

def setup_inputs():

    _w = (lambda a: (lambda t: t.to(torch.bfloat16) if t.is_floating_point() else t)(torch.as_tensor(a)))
    def _A_setup():
        B, S, D = 1, 2048, 640
        return {
            "x": _w((lambda *s: torch.randn(*s, dtype=torch.bfloat16) * 0.05)(B, S, D)),
            "wqkv": _w((lambda *s: torch.randn(*s, dtype=torch.bfloat16) * 0.05)(D, 3 * D)),
            "wo": _w((lambda *s: torch.randn(*s, dtype=torch.bfloat16) * 0.05)(D, D)),
        }
    _A_in = _A_setup()

    def _B_setup():
        B, S, D, F = 1, 2048, 640, 1536
        return {
            "x": _w((lambda *s: torch.randn(*s, dtype=torch.bfloat16) * 0.05)(B, S, D)),
            "w_gate": _w((lambda *s: torch.randn(*s, dtype=torch.bfloat16) * 0.05)(D, F)),
            "w_up": _w((lambda *s: torch.randn(*s, dtype=torch.bfloat16) * 0.05)(D, F)),
            "w_down": _w((lambda *s: torch.randn(*s, dtype=torch.bfloat16) * 0.05)(F, D)),
        }
    _B_in = _B_setup()

    return {
        "A_x": _A_in["x"],
        "A_wqkv": _A_in["wqkv"],
        "A_wo": _A_in["wo"],
        "B_x": _B_in["x"],
        "B_w_gate": _B_in["w_gate"],
        "B_w_up": _B_in["w_up"],
        "B_w_down": _B_in["w_down"],
    }

def reference(A_x, A_wqkv, A_wo, B_x, B_w_gate, B_w_up, B_w_down):
    def _A_ref(x, wqkv, wo):
        B, S, D = x.shape
        H = 32; Dh = D // H; W = 256
        qkv = (lambda x, *d: x.permute(*d))((x @ wqkv).reshape(B, S, 3, H, Dh), 2, 0, 3, 1, 4)
        q, k, v = qkv[0], qkv[1], qkv[2]
        scores = torch.einsum("bhqd,bhkd->bhqk", q, k) * (Dh ** -0.5)
        idx = torch.arange(S)
        mask = (idx[:, None] - idx[None, :] >= 0) & (idx[:, None] - idx[None, :] < W)
        scores = torch.where(mask[None, None], scores, -1e9)
        p = _softmax(scores, axis=-1)
        o = torch.einsum("bhqk,bhkd->bhqd", p, v)
        return (lambda x, *d: x.permute(*d))(o, 0, 2, 1, 3).reshape(B, S, D) @ wo

    def _B_ref(x, w_gate, w_up, w_down):
        g = _silu(x @ w_gate)
        u = x @ w_up
        return (g * u) @ w_down

    x = A_x
    xn = _layernorm(x, axis=-1)
    return x + _A_ref(xn, A_wqkv, A_wo) + _B_ref(xn, B_w_gate, B_w_up, B_w_down)
```

```cpp
#include <hip/hip_runtime.h>
#include <hip/hip_bf16.h>
typedef __hip_bfloat16 bf16;
constexpr int S=2048, D=640, H=32, DH=20, W=256, F=1536;

__global__ void ln_k(const bf16* x, float* xn){
  int row=blockIdx.x; __shared__ float red[256];
  float s=0; for(int i=threadIdx.x;i<D;i+=256) s+=__bfloat162float(x[row*D+i]);
  red[threadIdx.x]=s; __syncthreads();
  for(int o=128;o>0;o>>=1){ if(threadIdx.x<o) red[threadIdx.x]+=red[threadIdx.x+o]; __syncthreads(); }
  float m=red[0]/D; __syncthreads();
  float v=0; for(int i=threadIdx.x;i<D;i+=256){ float d=__bfloat162float(x[row*D+i])-m; v+=d*d; }
  red[threadIdx.x]=v; __syncthreads();
  for(int o=128;o>0;o>>=1){ if(threadIdx.x<o) red[threadIdx.x]+=red[threadIdx.x+o]; __syncthreads(); }
  float r=rsqrtf(red[0]/D+1e-5f);
  for(int i=threadIdx.x;i<D;i+=256) xn[row*D+i]=__bfloat162float(__float2bfloat16((__bfloat162float(x[row*D+i])-m)*r));
}
__global__ void gemm_k(const float* A,const bf16* B,float* C,int M,int N,int K){
  int n=blockIdx.x*64+(threadIdx.x&63), m=blockIdx.y*4+(threadIdx.x>>6);
  float acc=0; for(int k=0;k<K;k++) acc+=A[m*K+k]*__bfloat162float(B[k*N+n]);
  C[m*N+n]=__bfloat162float(__float2bfloat16(acc));
}
__global__ void attn_k(const float* qkv,float* o){
  int idx=blockIdx.x*256+threadIdx.x; int t=idx/H, h=idx%H;
  float q[DH]; for(int d=0;d<DH;d++) q[d]=qkv[t*3*D+h*DH+d];
  float m=-1e30f; int j0=max(0,t-W+1);
  for(int j=j0;j<=t;j++){ float s=0; for(int d=0;d<DH;d++) s+=q[d]*qkv[j*3*D+D+h*DH+d]; s*=0.22360679775f; m=fmaxf(m,s);}
  float l=0, acc[DH]; for(int d=0;d<DH;d++) acc[d]=0;
  for(int j=j0;j<=t;j++){ float s=0; for(int d=0;d<DH;d++) s+=q[d]*qkv[j*3*D+D+h*DH+d]; s*=0.22360679775f; float e=__expf(s-m); l+=e; for(int d=0;d<DH;d++) acc[d]+=e*qkv[j*3*D+2*D+h*DH+d]; }
  for(int d=0;d<DH;d++) o[t*D+h*DH+d]=__bfloat162float(__float2bfloat16(acc[d]/l));
}
__global__ void glu_k(const float* g,const float* u,float* h){
  int i=blockIdx.x*256+threadIdx.x; float a=g[i]; h[i]=__bfloat162float(__float2bfloat16(a/(1.f+__expf(-a))*u[i]));
}
__global__ void fin_k(const bf16* x,const float* a,const float* f,bf16* out){
  int i=blockIdx.x*256+threadIdx.x; out[i]=__float2bfloat16(__bfloat162float(x[i])+a[i]+f[i]);
}
extern "C" void kernel_launch(void* const* d_in, const int* in_sizes, int n_in,
                              void* d_out, int out_size, void* d_ws, size_t ws_size,
                              hipStream_t stream) {
  const bf16* x=(const bf16*)d_in[0]; const bf16* wqkv=(const bf16*)d_in[1]; const bf16* wo=(const bf16*)d_in[2];
  const bf16* wg=(const bf16*)d_in[4]; const bf16* wu=(const bf16*)d_in[5]; const bf16* wd=(const bf16*)d_in[6];
  float* ws=(float*)d_ws;
  float* xn=ws; float* qkv=xn+S*D; float* o=qkv+S*3*D; float* ao=o+S*D; float* g=ao+S*D; float* u=g+S*F; float* h=u+S*F; float* fo=h+S*F;
  ln_k<<<S,256,0,stream>>>(x,xn);
  gemm_k<<<dim3(3*D/64,S/4),256,0,stream>>>(xn,wqkv,qkv,S,3*D,D);
  gemm_k<<<dim3(F/64,S/4),256,0,stream>>>(xn,wg,g,S,F,D);
  gemm_k<<<dim3(F/64,S/4),256,0,stream>>>(xn,wu,u,S,F,D);
  attn_k<<<S*H/256,256,0,stream>>>(qkv,o);
  glu_k<<<S*F/256,256,0,stream>>>(g,u,h);
  gemm_k<<<dim3(D/64,S/4),256,0,stream>>>(o,wo,ao,S,D,D);
  gemm_k<<<dim3(D/64,S/4),256,0,stream>>>(h,wd,fo,S,D,F);
  fin_k<<<S*D/256,256,0,stream>>>(x,ao,fo,(bf16*)d_out);
}
```

```cpp
#include <hip/hip_runtime.h>
#include <hip/hip_bf16.h>
#include <stdint.h>

typedef short bf16x8 __attribute__((ext_vector_type(8)));
typedef short s16x4 __attribute__((ext_vector_type(4)));
typedef float f32x16 __attribute__((ext_vector_type(16)));
typedef float f32x4 __attribute__((ext_vector_type(4)));
typedef float f32x2 __attribute__((ext_vector_type(2)));
typedef unsigned u32x4 __attribute__((ext_vector_type(4)));
typedef unsigned u32x2 __attribute__((ext_vector_type(2)));
typedef __bf16 bf2 __attribute__((ext_vector_type(2)));
#define LAS __attribute__((address_space(3)))
#define DI __device__ __forceinline__

namespace {

constexpr int S_ = 2048, D_ = 640, F_ = 1536;

constexpr size_t OFF_CNT = 0;
constexpr size_t OFF_KQ = 4096;
constexpr size_t SZ_KQ = 320ull * 2048 * 8;
constexpr size_t OFF_VT = OFF_KQ + SZ_KQ;
constexpr int VT_TG = 21 * 32;
constexpr int VT_HEAD = 128 * VT_TG;
constexpr size_t SZ_VT = 32ull * VT_HEAD;
constexpr size_t OFF_HT = OFF_VT + SZ_VT;
constexpr size_t SZ_HT = 128ull * 48 * 1024;
constexpr size_t OFF_OT = OFF_HT + SZ_HT;
constexpr size_t SZ_OT = 128ull * 20 * 1024;
constexpr size_t OFF_W2 = OFF_OT + SZ_OT;

DI float swap_sum(float x) {
  auto r = __builtin_amdgcn_permlane32_swap(__float_as_uint(x), __float_as_uint(x), false, false);
  return __uint_as_float(r[0]) + __uint_as_float(r[1]);
}
DI float swap_max(float x) {
  auto r = __builtin_amdgcn_permlane32_swap(__float_as_uint(x), __float_as_uint(x), false, false);
  return fmaxf(__uint_as_float(r[0]), __uint_as_float(r[1]));
}
DI unsigned pk2(float a, float b) {
  f32x2 v = {a, b};
  return __builtin_bit_cast(unsigned, __builtin_convertvector(v, bf2));
}
DI float dot2(unsigned a, unsigned b, float c) {
  return __builtin_amdgcn_fdot2_f32_bf16(__builtin_bit_cast(bf2, a), __builtin_bit_cast(bf2, b), c, false);
}
DI void glds16(const char* g, LAS char* l) {
  __builtin_amdgcn_global_load_lds((const __attribute__((address_space(1))) void*)g, (LAS void*)l, 16, 0, 0);
}
DI s16x4 tr_read(LAS char* p) {
  return __builtin_amdgcn_ds_read_tr16_b64_v4i16((LAS s16x4*)p);
}
template <int N> DI void wait_bar() {
  asm volatile("s_waitcnt vmcnt(%0) lgkmcnt(0)\n\ts_barrier" ::"i"(N) : "memory");
}
template <int N> DI void wait_bar_nl() {
  asm volatile("s_waitcnt vmcnt(%0)\n\ts_barrier" ::"i"(N) : "memory");
}
#define MFMA32(a, b, c) __builtin_amdgcn_mfma_f32_32x32x16_bf16((a), (b), (c), 0, 0, 0)
#define MFMA16(a, b, c) __builtin_amdgcn_mfma_f32_16x16x32_bf16((a), (b), (c), 0, 0, 0)

constexpr int K1_WSLOT = 20480, K1_NWS = 5;
constexpr int K1_XOFF = K1_WSLOT * K1_NWS;
constexpr int K1_XSLOT = 16384, K1_NXS = 3;
constexpr int K1_CS = K1_XOFF + K1_XSLOT * K1_NXS;
constexpr int K1_ST = K1_CS + 1280;
constexpr int K1_LDS = K1_ST + 1024;

DI void retile_load(const char* __restrict__ wd, const char* __restrict__ wo, int item, u32x4 (&in)[8]) {
  const int G = item / 80, ng = item - G * 80;
  const char* src = (G < 192) ? (wd + (size_t)(8 * G) * 1280) : (wo + (size_t)(8 * G - 1536) * 1280);
  src += ng * 16;
#pragma unroll
  for (int i = 0; i < 8; ++i) in[i] = *(const u32x4*)(src + i * 1280);
}
DI void retile_store(char* __restrict__ ws, int item, const u32x4 (&in)[8]) {
  const int G = item / 80, ng = item - G * 80;
  const int n = 8 * ng, ct = n / 80, nn = n - ct * 80;
  char* dst = ws + OFF_W2 + ((((size_t)ct * 68 + (G >> 2)) * 5 + (nn >> 4)) * 64 + (nn & 15) + 16 * (G & 3)) * 16;
#pragma unroll
  for (int a = 0; a < 4; ++a) {
    u32x4 lo, hi;
#pragma unroll
    for (int b = 0; b < 4; ++b) {
      lo[b] = __builtin_amdgcn_perm(in[2 * b + 1][a], in[2 * b][a], 0x05040100u);
      hi[b] = __builtin_amdgcn_perm(in[2 * b + 1][a], in[2 * b][a], 0x07060302u);
    }
    *(u32x4*)(dst + (2 * a) * 16) = lo;
    *(u32x4*)(dst + (2 * a + 1) * 16) = hi;
  }
}
DI float bf_lo(unsigned v) { return __uint_as_float(v << 16); }
DI float bf_hi(unsigned v) { return __uint_as_float(v & 0xffff0000u); }
DI void bar_only() { asm volatile("s_barrier" ::: "memory"); }

template <class T> DI void st_nt(char* p, T v) { __builtin_nontemporal_store(v, (T*)p); }

template <int TYPE>
DI void k1_body(const char* __restrict__ x, const char* __restrict__ wqkv, const char* __restrict__ wg,
                const char* __restrict__ wu, const char* __restrict__ wd, const char* __restrict__ wo,
                char* __restrict__ ws, LAS char* lds, int rb, int nt) {
  constexpr int NU = (TYPE == 1) ? 4 : 5;
  const int tid = threadIdx.x, lane = tid & 63;
  const int w = __builtin_amdgcn_readfirstlane(tid >> 6);
  const int qcol0 = (TYPE == 0) ? nt * 64 : (TYPE == 1) ? (12 + 4 * (nt - 12)) * 64 : 1280 + 5 * (nt - 14) * 64;

  if (w >= 4) {
    const int hw = w - 4;
    const char* ub[5];
    if (TYPE == 0) {
      ub[0] = wg + (2 * nt) * 128; ub[1] = wu + (2 * nt) * 128;
      ub[2] = wg + (2 * nt + 1) * 128; ub[3] = wu + (2 * nt + 1) * 128;
      ub[4] = wqkv + qcol0 * 2;
    } else {
      for (int u = 0; u < 5; ++u) ub[u] = wqkv + (qcol0 + 64 * u) * 2;
    }
    unsigned voffA, voffB, xvoff0, xvoff1;
    {
      int row = 8 * hw + (lane >> 3);
      int cc = ((((lane >> 2) & 1) ^ ((row >> 1) & 1)) << 2) + (lane & 3);
      voffA = row * 3072 + cc * 16;
      voffB = row * 3840 + cc * 16;
      int xr = rb * 128 + 32 * hw + (lane >> 3);
      int sc = (lane & 7) ^ (lane >> 4);
      xvoff0 = xr * 1280 + sc * 16;
      xvoff1 = xr * 1280 + (sc ^ 4) * 16;
    }
    const unsigned ldsb = (unsigned)(size_t)lds;
    auto dma = [&](const char* sbase, unsigned voff, unsigned dst) {
      asm volatile("s_mov_b32 m0, %0\n\ts_nop 0\n\tglobal_load_lds_dwordx4 %1, %2" ::"s"(dst), "v"(voff), "s"(sbase) : "memory", "m0");
    };
    auto issueW = [&](int slot) {
      const unsigned dst = ldsb + (slot % K1_NWS) * K1_WSLOT + hw * 1024;
#pragma unroll
      for (int u = 0; u < NU; ++u) {
        const bool a = (TYPE == 0 && u < 4);
        dma(ub[u] + (size_t)slot * 32 * (a ? 3072 : 3840), a ? voffA : voffB, dst + u * 4096);
      }
    };
    auto issueX = [&](int c) {
      const unsigned dst = ldsb + K1_XOFF + (c % K1_NXS) * K1_XSLOT + hw * 4096;
#pragma unroll
      for (int i = 0; i < 4; ++i) dma(x + c * 128 + i * 8 * 1280, (i & 1) ? xvoff1 : xvoff0, dst + i * 1024);
    };
    issueW(0); issueX(0); issueW(1); issueW(2); issueX(1);
    {
      const int t = tid - 256;
      *(LAS float*)(lds + K1_CS + t * 4) = 0.f;
      if (t < 64) *(LAS float*)(lds + K1_CS + 1024 + t * 4) = 0.f;
    }
    wait_bar<2 * NU + 4>();
    issueW(3);
#pragma unroll
    for (int i = 0; i < 19; ++i) {
      const int nw = ((i + 3 < 19) ? (i + 3) : 19) - (i + 1);
      const bool hx = (i & 1) ? (i <= 15) : (i <= 16);
      const int cnt = NU * nw + (hx ? 4 : 0);
      if (cnt == 2 * NU + 4) wait_bar<2 * NU + 4>();
      else if (cnt == 2 * NU) wait_bar<2 * NU>();
      else if (cnt == NU) wait_bar<NU>();
      else wait_bar<0>();
      if (i + 4 < 20) issueW(i + 4);
      if ((i & 1) == 0 && (i / 2 + 2) < 10) issueX(i / 2 + 2);
    }
    if (tid - 256 < 85) {
      u32x4 rin[8];
      const int ritem = blockIdx.x * 85 + (tid - 256);
      retile_load(wd, wo, ritem, rin);
      retile_store(ws, ritem, rin);
    }
    bar_only();
    return;
  }

  const int tw = w & 1, ch = w >> 1;
  const int r = lane & 31, hh = lane >> 5;
  int xa[2][2];
  {
    int e = hh ^ ((r >> 1) & 7);
#pragma unroll
    for (int g = 0; g < 2; ++g)
#pragma unroll
      for (int ph = 0; ph < 2; ++ph) {
        int tg = 2 * tw + (g ^ ch), s = ph ^ tw;
        xa[g][ph] = K1_XOFF + (32 * tg + r) * 128 + (((2 * s) ^ e) << 4);
      }
  }
  int wa[2][3];
  {
    int G = lane >> 4, chh = G & 1, q = (lane & 15) >> 2, p = lane & 3;
#pragma unroll
    for (int ph = 0; ph < 2; ++ph) {
      int b0 = (8 * hh + q) * 128 + 32 * chh + 8 * p + (ph ^ tw) * 2048;
      wa[ph][0] = b0 + 64 * (0 ^ (q >> 1)) + ch * 8192;
      wa[ph][1] = b0 + 64 * (1 ^ (q >> 1)) + ch * 8192;
      wa[ph][2] = b0 + 64 * (ch ^ (q >> 1)) + 4 * 4096;
    }
  }
  f32x16 acc[2][5];
#pragma unroll
  for (int g = 0; g < 2; ++g)
#pragma unroll
    for (int i = 0; i < 5; ++i)
#pragma unroll
      for (int j = 0; j < 16; ++j) acc[g][i][j] = 0.f;
  f32x4 cs[5];
#pragma unroll
  for (int i = 0; i < 5; ++i) cs[i] = f32x4{0.f, 0.f, 0.f, 0.f};
  bf16x8 onep;
  {
    const unsigned o = ((lane & 15) == ((lane >> 4) & 1)) ? 0x3f803f80u : 0u;
    u32x4 t = {o, o, o, o};
    onep = __builtin_bit_cast(bf16x8, t);
  }
  float s0 = 0.f, s1 = 0.f, q0 = 0.f, q1 = 0.f;

  bf16x8 xf[2][2];
  s16x4 wl[5], wh[5];
  auto rdx = [&](int i, int ph, int g) {
    LAS char* xb = lds + ((i / 2) % K1_NXS) * K1_XSLOT;
    xf[ph][g] = *(LAS bf16x8*)(xb + (xa[g][ph] ^ ((i & 1) * 64)));
  };
  auto rdw = [&](int i, int ph, int c5) {
    LAS char* p = lds + (i % K1_NWS) * K1_WSLOT + ((c5 < 4) ? ((c5 >> 1) * 4096 + wa[ph][c5 & 1]) : wa[ph][2]);
    wl[c5] = tr_read(p);
    wh[c5] = tr_read(p + 512);
  };
  auto phase = [&](int ph, int ni, int nph, bool do_rd) {
#pragma unroll
    for (int k = 0; k < 2 * NU; ++k) {
      const int c5 = k >> 1, g = k & 1;
      bf16x8 wfr = __builtin_shufflevector(wl[c5], wh[c5], 0, 1, 2, 3, 4, 5, 6, 7);
      if (TYPE == 2) acc[g][c5] = MFMA32(xf[ph][g], wfr, acc[g][c5]);
      else acc[g][c5] = MFMA32(wfr, xf[ph][g], acc[g][c5]);
      if (ph == 0 && g == 0) cs[c5] = MFMA16(wfr, onep, cs[c5]);
      if (do_rd) {
        if (g == 1) rdw(ni, nph, c5);
        if (k == 0) rdx(ni, nph, 0);
        if (k == 2) rdx(ni, nph, 1);
      }
      if (k < 8) {
        u32x4 xu = __builtin_bit_cast(u32x4, xf[ph][0]);
        if (k & 1) { float h = bf_hi(xu[k >> 1]); s1 += h; q1 = fmaf(h, h, q1); asm volatile("" : "+v"(s1), "+v"(q1)); }
        else { float l = bf_lo(xu[k >> 1]); s0 += l; q0 = fmaf(l, l, q0); asm volatile("" : "+v"(s0), "+v"(q0)); }
      }
      __builtin_amdgcn_sched_barrier(0);
    }
  };

  bar_only();
  rdx(0, 0, 0); rdw(0, 0, 0); rdx(0, 0, 1);
#pragma unroll
  for (int c5 = 1; c5 < NU; ++c5) rdw(0, 0, c5);
  __builtin_amdgcn_sched_barrier(0);
#pragma unroll
  for (int i = 0; i < 20; ++i) {
    phase(0, i, 1, true);
    if (i < 19) {
      bar_only();
      __builtin_amdgcn_sched_barrier(0);
      phase(1, i + 1, 0, true);
    } else {
      phase(1, 0, 0, false);
    }
  }

  int tz = threadIdx.x;
  asm volatile("" : "+v"(tz));
  const int lane_ = tz & 63, r_ = tz & 31, hh_ = (tz >> 5) & 1;
#define lane lane_
#define r r_
#define hh hh_
  LAS float* csb = (LAS float*)(lds + K1_CS) + ch * 160;
  {
    const float ssum = swap_sum(s0 + s1), ssq = swap_sum(q0 + q1);
    const float mean = ssum * (1.f / 640.f);
    const float var = fmaxf(ssq * (1.f / 640.f) - mean * mean, 0.f);
    const float rs = __builtin_amdgcn_rsqf(var + 1e-5f);
    if (lane < 32) {
      f32x2 st = {-mean, rs};
      *(LAS f32x2*)(lds + K1_ST + (32 * (2 * tw + ch) + lane) * 8) = st;
    }
    if ((lane & 15) < 2) {
      LAS float* cp = csb + 16 * (lane & 15) + 4 * (lane >> 4);
#pragma unroll
      for (int c5 = 0; c5 < NU; ++c5)
#pragma unroll
        for (int j = 0; j < 4; ++j)
          __hip_atomic_fetch_add(cp + c5 * 32 + j, cs[c5][j], __ATOMIC_RELAXED, __HIP_MEMORY_SCOPE_WORKGROUP);
    }
  }
  asm volatile("s_waitcnt lgkmcnt(0)\n\ts_barrier" ::: "memory");

  if (TYPE != 2) {
    f32x2 st[2];
#pragma unroll
    for (int g = 0; g < 2; ++g) st[g] = *(LAS f32x2*)(lds + K1_ST + (32 * (2 * tw + (g ^ ch)) + r) * 8);
#pragma unroll
    for (int c5 = 0; c5 < NU; ++c5) {
      f32x4 c4[4];
#pragma unroll
      for (int q4 = 0; q4 < 4; ++q4) c4[q4] = *(LAS f32x4*)(csb + c5 * 32 + 8 * q4 + 4 * hh);
#pragma unroll
      for (int g = 0; g < 2; ++g) {
        const f32x2 nm2 = {st[g][0], st[g][0]};
#pragma unroll
        for (int q4 = 0; q4 < 4; ++q4)
#pragma unroll
          for (int j = 0; j < 2; ++j) {
            f32x2 v = {acc[g][c5][4 * q4 + 2 * j], acc[g][c5][4 * q4 + 2 * j + 1]};
            v = nm2 * f32x2{c4[q4][2 * j], c4[q4][2 * j + 1]} + v;
            acc[g][c5][4 * q4 + 2 * j] = v[0]; acc[g][c5][4 * q4 + 2 * j + 1] = v[1];
          }
      }
    }
#pragma unroll
    for (int g = 0; g < 2; ++g) {
      const int tok = rb * 128 + 32 * (2 * tw + (g ^ ch)) + r;
      const float rs = st[g][1];
      const f32x2 rs2 = {rs, rs};
      char* kq = ws + OFF_KQ + (size_t)tok * 8 + (size_t)hh * 16384;
      auto store_qk = [&](const f32x16& a, int nb) {
#pragma unroll
        for (int q4 = 0; q4 < 4; ++q4) {
          f32x2 v0 = f32x2{a[4 * q4], a[4 * q4 + 1]} * rs2, v1 = f32x2{a[4 * q4 + 2], a[4 * q4 + 3]} * rs2;
          u32x2 v = {pk2(v0[0], v0[1]), pk2(v1[0], v1[1])};
          st_nt(kq + (size_t)(nb / 4 + 2 * q4) * 16384, v);
        }
      };
      if (TYPE == 0) {
        store_qk(acc[g][4], qcol0 + 32 * ch);
        char* ht = ws + OFF_HT + ((size_t)(tok >> 4) * 48 * 64 + (tok & 15) + 16 * hh) * 16 + (size_t)(2 * (2 * nt + ch)) * 1024;
        const float k1 = -1.4426950408889634f * rs;
        const f32x2 k12 = {k1, k1};
        const float irs2 = __builtin_amdgcn_rcpf(rs * rs);
        const f32x2 ir2 = {irs2, irs2};
#pragma unroll
        for (int c = 0; c < 2; ++c) {
          unsigned P[8];
#pragma unroll
          for (int j = 0; j < 8; ++j) {
            f32x2 gg = {acc[g][c][2 * j], acc[g][c][2 * j + 1]};
            f32x2 uu = {acc[g][2 + c][2 * j], acc[g][2 + c][2 * j + 1]};
            f32x2 t = gg * k12;
            f32x2 e = {__builtin_amdgcn_exp2f(t[0]), __builtin_amdgcn_exp2f(t[1])};
            f32x2 d = e * ir2 + ir2;
            f32x2 rr = {__builtin_amdgcn_rcpf(d[0]), __builtin_amdgcn_rcpf(d[1])};
            f32x2 hv = (gg * uu) * rr;
            P[j] = pk2(hv[0], hv[1]);
          }
#pragma unroll
          for (int qp = 0; qp < 2; ++qp) {
            auto r0 = __builtin_amdgcn_permlane32_swap(P[4 * qp], P[4 * qp + 2], false, false);
            auto r1 = __builtin_amdgcn_permlane32_swap(P[4 * qp + 1], P[4 * qp + 3], false, false);
            u32x4 v = {r0[0], r1[0], r0[1], r1[1]};
            st_nt(ht + c * 1024 + qp * 512, v);
          }
        }
      } else {
#pragma unroll
        for (int c5 = 0; c5 < 4; ++c5) store_qk(acc[g][c5], qcol0 + 128 * ch + 32 * c5);
      }
    }
  } else {
    int voff[5];
    float cn[5];
#pragma unroll
    for (int c5 = 0; c5 < 5; ++c5) {
      const int nv = qcol0 - 1280 + ((c5 < 4) ? (128 * ch + 32 * c5) : (256 + 32 * ch)) + r;
      const int head = nv / 20, d = nv - head * 20;
      voff[c5] = head * VT_HEAD + hh * 336 + d * 16;
      cn[c5] = csb[c5 * 32 + r];
    }
#pragma unroll
    for (int g = 0; g < 2; ++g) {
      const int tb = 32 * (2 * tw + (g ^ ch));
      f32x2 mm[8], rr[8];
#pragma unroll
      for (int q4 = 0; q4 < 4; ++q4) {
        f32x4 a = *(LAS f32x4*)(lds + K1_ST + (tb + 8 * q4 + 4 * hh) * 8);
        f32x4 b = *(LAS f32x4*)(lds + K1_ST + (tb + 8 * q4 + 4 * hh) * 8 + 16);
        mm[2 * q4] = f32x2{a[0], a[2]}; rr[2 * q4] = f32x2{a[1], a[3]};
        mm[2 * q4 + 1] = f32x2{b[0], b[2]}; rr[2 * q4 + 1] = f32x2{b[1], b[3]};
      }
      char* vb = ws + OFF_VT + ((rb * 128 + tb) >> 4) * VT_TG;
#pragma unroll
      for (int c5 = 0; c5 < 5; ++c5) {
        const f32x2 c2 = {cn[c5], cn[c5]};
#pragma unroll
        for (int s = 0; s < 2; ++s) {
          u32x4 v;
#pragma unroll
          for (int j = 0; j < 4; ++j) {
            f32x2 t = {acc[g][c5][8 * s + 2 * j], acc[g][c5][8 * s + 2 * j + 1]};
            t = (mm[4 * s + j] * c2 + t) * rr[4 * s + j];
            v[j] = pk2(t[0], t[1]);
          }
          st_nt(vb + voff[c5] + s * VT_TG, v);
        }
      }
    }
  }
#undef lane
#undef r
#undef hh
}

__global__ __launch_bounds__(512, 2) void k1_kernel(const char* __restrict__ x, const char* __restrict__ wqkv,
                                                    const char* __restrict__ wo, const char* __restrict__ wg,
                                                    const char* __restrict__ wu, const char* __restrict__ wd,
                                                    char* __restrict__ ws) {
  __shared__ __attribute__((aligned(16))) char smem[K1_LDS];
  LAS char* lds = (LAS char*)smem;
  const int b = blockIdx.x, xcd = b & 7, idx = b >> 3;
  const int rb = 8 * (xcd & 1) + (idx & 7), nt = 4 * (xcd >> 1) + (idx >> 3);
  if (b == 0 && threadIdx.x < 8) *(unsigned*)(ws + OFF_CNT + threadIdx.x * 256) = 0u;
  if (nt < 12) k1_body<0>(x, wqkv, wg, wu, wd, wo, ws, lds, rb, nt);
  else if (nt < 14) k1_body<1>(x, wqkv, wg, wu, wd, wo, ws, lds, rb, nt);
  else k1_body<2>(x, wqkv, wg, wu, wd, wo, ws, lds, rb, nt);
}

constexpr int K2_K = 0;
constexpr int K2_Q = 20480;
constexpr int K2_V = 30720;
constexpr int K2_FLAG = K2_V + 21504;
constexpr int K2_RED = K2_FLAG + 64;
constexpr int RED_ROW = 336;
constexpr int K2_LDS = K2_RED + 4 * 64 * RED_ROW;

__global__ __launch_bounds__(512, 2) void k2_kernel(const char* __restrict__ x, char* __restrict__ ws,
                                                    char* __restrict__ out) {
  __shared__ __attribute__((aligned(16))) char smem[K2_LDS];
  LAS char* lds = (LAS char*)smem;
  const int tid = threadIdx.x, lane = tid & 63;
  const int w = __builtin_amdgcn_readfirstlane(tid >> 6);
  const int b = blockIdx.x, xcd = b & 7, idx = b >> 3;
  LAS unsigned* flg = (LAS unsigned*)(lds + K2_FLAG);
  if (tid < 2) flg[tid] = 0u;
  asm volatile("s_waitcnt lgkmcnt(0)\n\ts_barrier" ::: "memory");

  if (w < 4) {
    const int head = 4 * xcd + (idx & 3), rb = idx >> 2;
    const int r = lane & 31, hh = lane >> 5;
    {
      const char* kq = ws + OFF_KQ + lane * 16;
      for (int j = w; j < 20; j += 4) {
        const int dg = j >> 2, qc = j & 3;
        if (rb > 0 || qc >= 2)
          glds16(kq + (size_t)(160 + head * 5 + dg) * 16384 + (256 * rb - 256 + 128 * qc) * 8, lds + K2_K + j * 1024);
      }
      for (int j = w; j < 10; j += 4) {
        const int dg = j >> 1, qc = j & 1;
        glds16(kq + (size_t)(head * 5 + dg) * 16384 + (256 * rb + 128 * qc) * 8, lds + K2_Q + j * 1024);
      }
      const char* vt = ws + OFF_VT + (size_t)head * VT_HEAD + (16 * rb - 16) * VT_TG + lane * 16;
      for (int j = w; j < 21; j += 4) {
        if (rb > 0 || j >= 11) glds16(vt + j * 1024, lds + K2_V + j * 1024);
        else if (j == 10) { if (lane >= 32) glds16(vt + j * 1024, lds + K2_V + j * 1024); }
      }
    }
    asm volatile("s_waitcnt vmcnt(0)" ::: "memory");
    {
      const int o1 = (lane >> 1) * VT_TG + (lane & 1) * 336 + 320;
      if (((o1 >> 10) & 3) == w) {
        u32x4 one = {0x3f803f80u, 0x3f803f80u, 0x3f803f80u, 0x3f803f80u};
        *(LAS u32x4*)(lds + K2_V + o1) = one;
      }
      asm volatile("s_waitcnt lgkmcnt(0)" ::: "memory");
    }
    if (lane == 0) __hip_atomic_fetch_add(flg, 1u, __ATOMIC_RELAXED, __HIP_MEMORY_SCOPE_WORKGROUP);
    for (int sp = 0; sp < (1 << 22); ++sp) {
      if (__hip_atomic_load(flg, __ATOMIC_RELAXED, __HIP_MEMORY_SCOPE_WORKGROUP) >= 4u) break;
      __builtin_amdgcn_s_sleep(1);
    }
    asm volatile("" ::: "memory");

    constexpr float C2 = 0.22360679774997896f * 1.4426950408889634f;
    const int dcl = (r < 20) ? r : 20;
#pragma unroll 1
    for (int qi = 0; qi < 2; ++qi) {
      const int qt = 2 * w + qi;
      const int tmin = (rb == 0) ? (8 - qt) : 0;
      bf16x8 q0, q1;
      {
        LAS char* qp = lds + K2_Q + (32 * qt + r) * 8;
        u32x2 a = *(LAS u32x2*)(qp + (2 * hh) * 2048), bb = *(LAS u32x2*)(qp + (2 * hh + 1) * 2048);
        u32x2 c = *(LAS u32x2*)(qp + 4 * 2048);
        u32x4 t0 = {a[0], a[1], bb[0], bb[1]};
        u32x4 t1 = {hh ? 0u : c[0], hh ? 0u : c[1], 0u, 0u};
        q0 = __builtin_bit_cast(bf16x8, t0); q1 = __builtin_bit_cast(bf16x8, t1);
      }
      LAS char* kp = lds + K2_K + (32 * qt + r) * 8;
      f32x16 sc[9];
#pragma unroll
      for (int t = 0; t < 9; ++t) {
        if (t >= tmin) {
          u32x2 a = *(LAS u32x2*)(kp + (2 * hh) * 4096 + t * 256), bb = *(LAS u32x2*)(kp + (2 * hh + 1) * 4096 + t * 256);
          u32x2 c = *(LAS u32x2*)(kp + 4 * 4096 + t * 256);
          u32x4 t0 = {a[0], a[1], bb[0], bb[1]};
          u32x4 t1 = {c[0], c[1], 0u, 0u};
          f32x16 z;
#pragma unroll
          for (int j = 0; j < 16; ++j) z[j] = 0.f;
          z = MFMA32(__builtin_bit_cast(bf16x8, t0), q0, z);
          sc[t] = MFMA32(__builtin_bit_cast(bf16x8, t1), q1, z);
        } else {
#pragma unroll
          for (int j = 0; j < 16; ++j) sc[t][j] = -INFINITY;
        }
      }
#pragma unroll
      for (int j = 0; j < 16; ++j) {
        const int kl = (j & 3) + 8 * (j >> 2) + 4 * hh;
        if (!(kl > r)) sc[0][j] = -INFINITY;
        if (!(kl <= r)) sc[8][j] = -INFINITY;
      }
      float m = sc[8][0];
#pragma unroll
      for (int t = 0; t < 9; ++t)
#pragma unroll
        for (int j = 0; j < 16; j += 2) m = fmaxf(fmaxf(m, sc[t][j]), sc[t][j + 1]);
      m = swap_max(m);
      const float nm = -m * C2;
      LAS char* vp = lds + K2_V + (2 * qt) * VT_TG + dcl * 16 + hh * 336;
      f32x16 o;
#pragma unroll
      for (int j = 0; j < 16; ++j) o[j] = 0.f;
#pragma unroll
      for (int t = 0; t < 9; ++t) {
        if (t >= tmin) {
#pragma unroll
          for (int s = 0; s < 2; ++s) {
            float p[8];
#pragma unroll
            for (int j = 0; j < 8; ++j) p[j] = __builtin_amdgcn_exp2f(fmaf(sc[t][8 * s + j], C2, nm));
            u32x4 pp = {pk2(p[0], p[1]), pk2(p[2], p[3]), pk2(p[4], p[5]), pk2(p[6], p[7])};
            bf16x8 vf = *(LAS bf16x8*)(vp + (2 * t + s) * VT_TG);
            o = MFMA32(vf, __builtin_bit_cast(bf16x8, pp), o);
          }
        }
      }
      const float rl = __builtin_amdgcn_rcpf(o[12]);
      const int tok = 256 * rb + 32 * qt + r;
      char* ob = ws + OFF_OT + ((size_t)(tok >> 4) * 20 * 64 + (tok & 15)) * 16;
#pragma unroll
      for (int a = 0; a < 3; ++a) {
        const int k = head * 20 + 8 * a + 4 * hh;
        unsigned long long v = (unsigned long long)pk2(o[4 * a] * rl, o[4 * a + 1] * rl) |
                               ((unsigned long long)pk2(o[4 * a + 2] * rl, o[4 * a + 3] * rl) << 32);
        char* p = ob + (size_t)(k >> 5) * 1024 + ((k >> 3) & 3) * 256 + (k & 7) * 2;
        if (a < 2 || hh == 0)
          __hip_atomic_store((unsigned long long*)p, v, __ATOMIC_RELAXED, __HIP_MEMORY_SCOPE_AGENT);
      }
    }
    asm volatile("s_waitcnt vmcnt(0)" ::: "memory");
    if (lane == 0) {
      unsigned old = __hip_atomic_fetch_add(flg + 1, 1u, __ATOMIC_RELAXED, __HIP_MEMORY_SCOPE_WORKGROUP);
      if (old == 3u)
        __hip_atomic_fetch_add((unsigned*)(ws + OFF_CNT + rb * 256), 1u, __ATOMIC_RELAXED, __HIP_MEMORY_SCOPE_AGENT);
    }
    return;
  }

  const int kw = w - 4;
  const int rb64 = 8 * (xcd >> 1) + (idx & 7), ct = 4 * (xcd & 1) + (idx >> 3);
  const char* wp = ws + OFF_W2 + (size_t)ct * 68 * 5 * 1024 + lane * 16;
  const char* hp = ws + OFF_HT + (size_t)(4 * rb64) * 48 * 1024 + lane * 16;
  auto orsrc = __builtin_amdgcn_make_buffer_rsrc((void*)(ws + OFF_OT), 0, (int)SZ_OT, 0x00020000);
  const int ovoff = (4 * rb64) * 20 * 1024 + lane * 16;
  f32x4 acc[5][4];
#pragma unroll
  for (int n = 0; n < 5; ++n)
#pragma unroll
    for (int i = 0; i < 4; ++i) acc[n][i] = f32x4{0.f, 0.f, 0.f, 0.f};
  bf16x8 bw[3][5], bt[3][4];
  auto load = [&](int st, int sl) {
    if (st < 12) {
      const int ks = 12 * kw + st;
#pragma unroll
      for (int n = 0; n < 5; ++n) bw[sl][n] = *(const bf16x8*)(wp + (size_t)(ks * 5 + n) * 1024);
#pragma unroll
      for (int i = 0; i < 4; ++i) bt[sl][i] = *(const bf16x8*)(hp + (size_t)(i * 48 + ks) * 1024);
    } else {
      const int ko = 5 * kw + (st - 12);
#pragma unroll
      for (int n = 0; n < 5; ++n) bw[sl][n] = *(const bf16x8*)(wp + (size_t)((48 + ko) * 5 + n) * 1024);
#pragma unroll
      for (int i = 0; i < 4; ++i)
        bt[sl][i] = __builtin_bit_cast(bf16x8, __builtin_amdgcn_raw_buffer_load_b128(orsrc, ovoff + (i * 20 + ko) * 1024, 0, 16));
    }
  };
  load(0, 0); load(1, 1);
#pragma unroll
  for (int st = 0; st < 17; ++st) {
    if (st + 2 == 12) {
      const unsigned* cnt = (const unsigned*)(ws + OFF_CNT + (rb64 >> 2) * 256);
      for (int sp = 0; sp < (1 << 22); ++sp) {
        if (__hip_atomic_load(cnt, __ATOMIC_RELAXED, __HIP_MEMORY_SCOPE_AGENT) >= 32u) break;
        __builtin_amdgcn_s_sleep(2);
      }
      __builtin_amdgcn_fence(__ATOMIC_ACQUIRE, "wavefront");
      asm volatile("" ::: "memory");
    }
    if (st + 2 < 17) load(st + 2, (st + 2) % 3);
#pragma unroll
    for (int n = 0; n < 5; ++n)
#pragma unroll
      for (int i = 0; i < 4; ++i) acc[n][i] = MFMA16(bw[st % 3][n], bt[st % 3][i], acc[n][i]);
  }
  const int t2 = tid - 256;
  u32x4 xv[3];
#pragma unroll
  for (int it = 0; it < 3; ++it) {
    const int item = t2 + 256 * it;
    const int tk = item / 10, n8 = item - tk * 10;
    if (item < 640) xv[it] = *(const u32x4*)(x + (size_t)(64 * rb64 + tk) * 1280 + (80 * ct + 8 * n8) * 2);
  }
  {
    LAS char* rp = lds + K2_RED + (kw * 64 + (lane & 15)) * RED_ROW + (lane >> 4) * 16;
#pragma unroll
    for (int n = 0; n < 5; ++n)
#pragma unroll
      for (int i = 0; i < 4; ++i) *(LAS f32x4*)(rp + i * 16 * RED_ROW + n * 64) = acc[n][i];
  }
  asm volatile("s_waitcnt lgkmcnt(0)\n\ts_barrier" ::: "memory");
#pragma unroll
  for (int it = 0; it < 3; ++it) {
    const int item = t2 + 256 * it;
    const int tk = item / 10, n8 = item - tk * 10;
    if (item < 640) {
      LAS char* rp = lds + K2_RED + tk * RED_ROW + n8 * 32;
      f32x4 a = *(LAS f32x4*)rp, c = *(LAS f32x4*)(rp + 16);
#pragma unroll
      for (int k = 1; k < 4; ++k) {
        a += *(LAS f32x4*)(rp + k * 64 * RED_ROW);
        c += *(LAS f32x4*)(rp + k * 64 * RED_ROW + 16);
      }
      u32x4 xx = xv[it];
      u32x4 v;
      v[0] = pk2(a[0] + __uint_as_float(xx[0] << 16), a[1] + __uint_as_float(xx[0] & 0xffff0000u));
      v[1] = pk2(a[2] + __uint_as_float(xx[1] << 16), a[3] + __uint_as_float(xx[1] & 0xffff0000u));
      v[2] = pk2(c[0] + __uint_as_float(xx[2] << 16), c[1] + __uint_as_float(xx[2] & 0xffff0000u));
      v[3] = pk2(c[2] + __uint_as_float(xx[3] << 16), c[3] + __uint_as_float(xx[3] & 0xffff0000u));
      *(u32x4*)(out + (size_t)(64 * rb64 + tk) * 1280 + (80 * ct + 8 * n8) * 2) = v;
    }
  }
}

}

extern "C" void kernel_launch(void* const* d_in, const int* in_sizes, int n_in, void* d_out, int out_size,
                              void* d_ws, size_t ws_size, hipStream_t stream) {
  const char* x = (const char*)d_in[0];
  const char* wqkv = (const char*)d_in[1];
  const char* wo = (const char*)d_in[2];
  const char* wg = (const char*)d_in[4];
  const char* wu = (const char*)d_in[5];
  const char* wd = (const char*)d_in[6];
  k1_kernel<<<256, 512, 0, stream>>>(x, wqkv, wo, wg, wu, wd, (char*)d_ws);
  k2_kernel<<<256, 512, 0, stream>>>(x, (char*)d_ws, (char*)d_out);
}
```

```cpp
#include <hip/hip_runtime.h>
#include <hip/hip_bf16.h>
#include <stdint.h>

typedef short bf16x8 __attribute__((ext_vector_type(8)));
typedef short s16x4 __attribute__((ext_vector_type(4)));
typedef float f32x16 __attribute__((ext_vector_type(16)));
typedef float f32x4 __attribute__((ext_vector_type(4)));
typedef float f32x2 __attribute__((ext_vector_type(2)));
typedef unsigned u32x4 __attribute__((ext_vector_type(4)));
typedef unsigned u32x2 __attribute__((ext_vector_type(2)));
typedef __bf16 bf2 __attribute__((ext_vector_type(2)));
#define LAS __attribute__((address_space(3)))
#define DI __device__ __forceinline__

namespace {

constexpr int S_ = 2048, D_ = 640, F_ = 1536;
constexpr float QSCALE = 0.22360679774997896f * 1.4426950408889634f;

constexpr size_t OFF_CNT = 0;
constexpr size_t OFF_KQ = 4096;
constexpr size_t SZ_KQ = 64ull * 3 * 2048 * 16;
constexpr size_t OFF_VT = OFF_KQ + SZ_KQ;
constexpr int VT_TG = 21 * 32;
constexpr int VT_HEAD = 128 * VT_TG;
constexpr size_t SZ_VT = 32ull * VT_HEAD;
constexpr size_t OFF_HT = OFF_VT + SZ_VT;
constexpr size_t SZ_HT = 128ull * 48 * 1024;
constexpr size_t OFF_OT = OFF_HT + SZ_HT;
constexpr size_t SZ_OT = 128ull * 20 * 1024;
constexpr size_t OFF_W2 = OFF_OT + SZ_OT;

DI float swap_sum(float x) {
  auto r = __builtin_amdgcn_permlane32_swap(__float_as_uint(x), __float_as_uint(x), false, false);
  return __uint_as_float(r[0]) + __uint_as_float(r[1]);
}
DI float swap_max(float x) {
  auto r = __builtin_amdgcn_permlane32_swap(__float_as_uint(x), __float_as_uint(x), false, false);
  return fmaxf(__uint_as_float(r[0]), __uint_as_float(r[1]));
}
DI unsigned pk2(float a, float b) {
  f32x2 v = {a, b};
  return __builtin_bit_cast(unsigned, __builtin_convertvector(v, bf2));
}
DI float dot2(unsigned a, unsigned b, float c) {
  return __builtin_amdgcn_fdot2_f32_bf16(__builtin_bit_cast(bf2, a), __builtin_bit_cast(bf2, b), c, false);
}
DI void glds16(const char* g, LAS char* l) {
  __builtin_amdgcn_global_load_lds((const __attribute__((address_space(1))) void*)g, (LAS void*)l, 16, 0, 0);
}
DI s16x4 tr_read(LAS char* p) {
  return __builtin_amdgcn_ds_read_tr16_b64_v4i16((LAS s16x4*)p);
}
template <int N> DI void wait_bar() {
  asm volatile("s_waitcnt vmcnt(%0) lgkmcnt(0)\n\ts_barrier" ::"i"(N) : "memory");
}
template <int N> DI void wait_bar_nl() {
  asm volatile("s_waitcnt vmcnt(%0)\n\ts_barrier" ::"i"(N) : "memory");
}
#define MFMA32(a, b, c) __builtin_amdgcn_mfma_f32_32x32x16_bf16((a), (b), (c), 0, 0, 0)
#define MFMA16(a, b, c) __builtin_amdgcn_mfma_f32_16x16x32_bf16((a), (b), (c), 0, 0, 0)

constexpr int K1_WSLOT = 20480, K1_NWS = 5;
constexpr int K1_XOFF = K1_WSLOT * K1_NWS;
constexpr int K1_XSLOT = 16384, K1_NXS = 3;
constexpr int K1_CS = K1_XOFF + K1_XSLOT * K1_NXS;
constexpr int K1_ST = K1_CS + 1280;
constexpr int K1_LDS = K1_ST + 1024;

DI void retile_load(const char* __restrict__ wd, const char* __restrict__ wo, int item, u32x4 (&in)[8]) {
  const int G = item / 80, ng = item - G * 80;
  const char* src = (G < 192) ? (wd + (size_t)(8 * G) * 1280) : (wo + (size_t)(8 * G - 1536) * 1280);
  src += ng * 16;
#pragma unroll
  for (int i = 0; i < 8; ++i) in[i] = *(const u32x4*)(src + i * 1280);
}
DI void retile_store(char* __restrict__ ws, int item, const u32x4 (&in)[8]) {
  const int G = item / 80, ng = item - G * 80;
  const int n = 8 * ng, ct = n / 80, nn = n - ct * 80;
  char* dst = ws + OFF_W2 + ((((size_t)ct * 68 + (G >> 2)) * 5 + (nn >> 4)) * 64 + (nn & 15) + 16 * (G & 3)) * 16;
#pragma unroll
  for (int a = 0; a < 4; ++a) {
    u32x4 lo, hi;
#pragma unroll
    for (int b = 0; b < 4; ++b) {
      lo[b] = __builtin_amdgcn_perm(in[2 * b + 1][a], in[2 * b][a], 0x05040100u);
      hi[b] = __builtin_amdgcn_perm(in[2 * b + 1][a], in[2 * b][a], 0x07060302u);
    }
    *(u32x4*)(dst + (2 * a) * 16) = lo;
    *(u32x4*)(dst + (2 * a + 1) * 16) = hi;
  }
}
DI float bf_lo(unsigned v) { return __uint_as_float(v << 16); }
DI float bf_hi(unsigned v) { return __uint_as_float(v & 0xffff0000u); }
DI void bar_only() { asm volatile("s_barrier" ::: "memory"); }

template <class T> DI void st_nt(char* p, T v) { *(T*)p = v; }

template <int TYPE>
DI void k1_body(const char* __restrict__ x, const char* __restrict__ wqkv, const char* __restrict__ wg,
                const char* __restrict__ wu, const char* __restrict__ wd, const char* __restrict__ wo,
                char* __restrict__ ws, LAS char* lds, int rb, int nt) {
  constexpr int NU = (TYPE == 1) ? 4 : 5;
  const int tid = threadIdx.x, lane = tid & 63;
  const int w = __builtin_amdgcn_readfirstlane(tid >> 6);
  const int qcol0 = (TYPE == 0) ? nt * 64 : (TYPE == 1) ? (12 + 4 * (nt - 12)) * 64 : 1280 + 5 * (nt - 14) * 64;

  if (w >= 4) {
    const int hw = w - 4;
    const char* ub[5];
    if (TYPE == 0) {
      ub[0] = wg + (2 * nt) * 128; ub[1] = wu + (2 * nt) * 128;
      ub[2] = wg + (2 * nt + 1) * 128; ub[3] = wu + (2 * nt + 1) * 128;
      ub[4] = wqkv + qcol0 * 2;
    } else {
      for (int u = 0; u < 5; ++u) ub[u] = wqkv + (qcol0 + 64 * u) * 2;
    }
    unsigned voffA, voffB, xvoff0, xvoff1;
    {
      int row = 8 * hw + (lane >> 3);
      int cc = ((((lane >> 2) & 1) ^ ((row >> 1) & 1)) << 2) + (lane & 3);
      voffA = row * 3072 + cc * 16;
      voffB = row * 3840 + cc * 16;
      int xr = rb * 128 + 32 * hw + (lane >> 3);
      int sc = (lane & 7) ^ (lane >> 4);
      xvoff0 = xr * 1280 + sc * 16;
      xvoff1 = xr * 1280 + (sc ^ 4) * 16;
    }
    const unsigned ldsb = (unsigned)(size_t)lds;
    auto dma = [&](const char* sbase, unsigned voff, unsigned dst) {
      asm volatile("s_mov_b32 m0, %0\n\ts_nop 0\n\tglobal_load_lds_dwordx4 %1, %2" ::"s"(dst), "v"(voff), "s"(sbase) : "memory", "m0");
    };
    auto issueW = [&](int slot) {
      const unsigned dst = ldsb + (slot % K1_NWS) * K1_WSLOT + hw * 1024;
#pragma unroll
      for (int u = 0; u < NU; ++u) {
        const bool a = (TYPE == 0 && u < 4);
        dma(ub[u] + (size_t)slot * 32 * (a ? 3072 : 3840), a ? voffA : voffB, dst + u * 4096);
      }
    };
    auto issueX = [&](int c) {
      const unsigned dst = ldsb + K1_XOFF + (c % K1_NXS) * K1_XSLOT + hw * 4096;
#pragma unroll
      for (int i = 0; i < 4; ++i) dma(x + c * 128 + i * 8 * 1280, (i & 1) ? xvoff1 : xvoff0, dst + i * 1024);
    };
    issueW(0); issueX(0); issueW(1); issueW(2); issueX(1);
    {
      const int t = tid - 256;
      *(LAS float*)(lds + K1_CS + t * 4) = 0.f;
      if (t < 64) *(LAS float*)(lds + K1_CS + 1024 + t * 4) = 0.f;
    }
    wait_bar<2 * NU + 4>();
    issueW(3);
#pragma unroll
    for (int i = 0; i < 19; ++i) {
      const int nw = ((i + 3 < 19) ? (i + 3) : 19) - (i + 1);
      const bool hx = (i & 1) ? (i <= 15) : (i <= 16);
      const int cnt = NU * nw + (hx ? 4 : 0);
      if (cnt == 2 * NU + 4) wait_bar<2 * NU + 4>();
      else if (cnt == 2 * NU) wait_bar<2 * NU>();
      else if (cnt == NU) wait_bar<NU>();
      else wait_bar<0>();
      if (i + 4 < 20) issueW(i + 4);
      if ((i & 1) == 0 && (i / 2 + 2) < 10) issueX(i / 2 + 2);
    }
    if (tid - 256 < 85) {
      u32x4 rin[8];
      const int ritem = blockIdx.x * 85 + (tid - 256);
      retile_load(wd, wo, ritem, rin);
      retile_store(ws, ritem, rin);
    }
    bar_only();
    return;
  }

  const int tw = w & 1, ch = w >> 1;
  const int r = lane & 31, hh = lane >> 5;
  int xa[2][2];
  {
    int e = hh ^ ((r >> 1) & 7);
#pragma unroll
    for (int g = 0; g < 2; ++g)
#pragma unroll
      for (int ph = 0; ph < 2; ++ph) {
        int tg = 2 * tw + (g ^ ch), s = ph ^ tw;
        xa[g][ph] = K1_XOFF + (32 * tg + r) * 128 + (((2 * s) ^ e) << 4);
      }
  }
  int wa[2][3];
  {
    int G = lane >> 4, chh = G & 1, q = (lane & 15) >> 2, p = lane & 3;
#pragma unroll
    for (int ph = 0; ph < 2; ++ph) {
      int b0 = (8 * hh + q) * 128 + 32 * chh + 8 * p + (ph ^ tw) * 2048;
      wa[ph][0] = b0 + 64 * (0 ^ (q >> 1)) + ch * 8192;
      wa[ph][1] = b0 + 64 * (1 ^ (q >> 1)) + ch * 8192;
      wa[ph][2] = b0 + 64 * (ch ^ (q >> 1)) + 4 * 4096;
    }
  }
  f32x16 acc[2][5];
#pragma unroll
  for (int g = 0; g < 2; ++g)
#pragma unroll
    for (int i = 0; i < 5; ++i)
#pragma unroll
      for (int j = 0; j < 16; ++j) acc[g][i][j] = 0.f;
  f32x4 cs[5];
#pragma unroll
  for (int i = 0; i < 5; ++i) cs[i] = f32x4{0.f, 0.f, 0.f, 0.f};
  bf16x8 onep;
  {
    const unsigned o = ((lane & 15) == ((lane >> 4) & 1)) ? 0x3f803f80u : 0u;
    u32x4 t = {o, o, o, o};
    onep = __builtin_bit_cast(bf16x8, t);
  }
  float s0 = 0.f, s1 = 0.f, q0 = 0.f, q1 = 0.f;

  bf16x8 xf[2][2];
  s16x4 wl[5], wh[5];
  auto rdx = [&](int i, int ph, int g) {
    LAS char* xb = lds + ((i / 2) % K1_NXS) * K1_XSLOT;
    xf[ph][g] = *(LAS bf16x8*)(xb + (xa[g][ph] ^ ((i & 1) * 64)));
  };
  auto rdw = [&](int i, int ph, int c5) {
    LAS char* p = lds + (i % K1_NWS) * K1_WSLOT + ((c5 < 4) ? ((c5 >> 1) * 4096 + wa[ph][c5 & 1]) : wa[ph][2]);
    wl[c5] = tr_read(p);
    wh[c5] = tr_read(p + 512);
  };
  auto phase = [&](int ph, int ni, int nph, bool do_rd) {
#pragma unroll
    for (int k = 0; k < 2 * NU; ++k) {
      const int c5 = k >> 1, g = k & 1;
      bf16x8 wfr = __builtin_shufflevector(wl[c5], wh[c5], 0, 1, 2, 3, 4, 5, 6, 7);
      if (TYPE == 2) acc[g][c5] = MFMA32(xf[ph][g], wfr, acc[g][c5]);
      else acc[g][c5] = MFMA32(wfr, xf[ph][g], acc[g][c5]);
      if (ph == 0 && g == 0) cs[c5] = MFMA16(wfr, onep, cs[c5]);
      if (do_rd) {
        if (g == 1) rdw(ni, nph, c5);
        if (k == 0) rdx(ni, nph, 0);
        if (k == 2) rdx(ni, nph, 1);
      }
      if (k < 8) {
        u32x4 xu = __builtin_bit_cast(u32x4, xf[ph][0]);
        if (k & 1) { float h = bf_hi(xu[k >> 1]); s1 += h; q1 = fmaf(h, h, q1); asm volatile("" : "+v"(s1), "+v"(q1)); }
        else { float l = bf_lo(xu[k >> 1]); s0 += l; q0 = fmaf(l, l, q0); asm volatile("" : "+v"(s0), "+v"(q0)); }
      }
      __builtin_amdgcn_sched_barrier(0);
    }
  };

  bar_only();
  rdx(0, 0, 0); rdw(0, 0, 0); rdx(0, 0, 1);
#pragma unroll
  for (int c5 = 1; c5 < NU; ++c5) rdw(0, 0, c5);
  __builtin_amdgcn_sched_barrier(0);
#pragma unroll
  for (int i = 0; i < 20; ++i) {
    phase(0, i, 1, true);
    if (i < 19) {
      bar_only();
      __builtin_amdgcn_sched_barrier(0);
      phase(1, i + 1, 0, true);
    } else {
      phase(1, 0, 0, false);
    }
  }

  int tz = threadIdx.x;
  asm volatile("" : "+v"(tz));
  const int lane_ = tz & 63, r_ = tz & 31, hh_ = (tz >> 5) & 1;
#define lane lane_
#define r r_
#define hh hh_
  LAS float* csb = (LAS float*)(lds + K1_CS) + ch * 160;
  {
    const float ssum = swap_sum(s0 + s1), ssq = swap_sum(q0 + q1);
    const float mean = ssum * (1.f / 640.f);
    const float var = fmaxf(ssq * (1.f / 640.f) - mean * mean, 0.f);
    const float rs = __builtin_amdgcn_rsqf(var + 1e-5f);
    if (lane < 32) {
      f32x2 st = {-mean, rs};
      *(LAS f32x2*)(lds + K1_ST + (32 * (2 * tw + ch) + lane) * 8) = st;
    }
    if ((lane & 15) < 2) {
      LAS float* cp = csb + 16 * (lane & 15) + 4 * (lane >> 4);
#pragma unroll
      for (int c5 = 0; c5 < NU; ++c5)
#pragma unroll
        for (int j = 0; j < 4; ++j)
          __hip_atomic_fetch_add(cp + c5 * 32 + j, cs[c5][j], __ATOMIC_RELAXED, __HIP_MEMORY_SCOPE_WORKGROUP);
    }
  }
  asm volatile("s_waitcnt lgkmcnt(0)\n\ts_barrier" ::: "memory");

  if (TYPE != 2) {
    f32x2 st[2];
#pragma unroll
    for (int g = 0; g < 2; ++g) st[g] = *(LAS f32x2*)(lds + K1_ST + (32 * (2 * tw + (g ^ ch)) + r) * 8);
#pragma unroll
    for (int c5 = 0; c5 < NU; ++c5) {
      f32x4 c4[4];
#pragma unroll
      for (int q4 = 0; q4 < 4; ++q4) c4[q4] = *(LAS f32x4*)(csb + c5 * 32 + 8 * q4 + 4 * hh);
#pragma unroll
      for (int g = 0; g < 2; ++g) {
        const f32x2 nm2 = {st[g][0], st[g][0]};
#pragma unroll
        for (int q4 = 0; q4 < 4; ++q4)
#pragma unroll
          for (int j = 0; j < 2; ++j) {
            f32x2 v = {acc[g][c5][4 * q4 + 2 * j], acc[g][c5][4 * q4 + 2 * j + 1]};
            v = nm2 * f32x2{c4[q4][2 * j], c4[q4][2 * j + 1]} + v;
            acc[g][c5][4 * q4 + 2 * j] = v[0]; acc[g][c5][4 * q4 + 2 * j + 1] = v[1];
          }
      }
    }
#pragma unroll
    for (int g = 0; g < 2; ++g) {
      const int tok = rb * 128 + 32 * (2 * tw + (g ^ ch)) + r;
      const float rs = st[g][1];
      char* kq = ws + OFF_KQ + (size_t)tok * 16;
      auto store_qk = [&](const f32x16& a, int nb) {
        const float rsq = (nb < 640) ? rs * QSCALE : rs;
        const f32x2 rs2 = {rsq, rsq};
#pragma unroll
        for (int q4 = 0; q4 < 4; ++q4) {
          f32x2 v0 = f32x2{a[4 * q4], a[4 * q4 + 1]} * rs2, v1 = f32x2{a[4 * q4 + 2], a[4 * q4 + 3]} * rs2;
          u32x2 v = {pk2(v0[0], v0[1]), pk2(v1[0], v1[1])};
          const int i4 = nb / 4 + 2 * q4 + hh;
          const int hd = (i4 * 205) >> 10, dg = i4 - 5 * hd;
          st_nt(kq + (size_t)(hd * 3 + (dg >> 1)) * 32768 + (dg & 1) * 8, v);
        }
      };
      if (TYPE == 0) {
        store_qk(acc[g][4], qcol0 + 32 * ch);
        char* ht = ws + OFF_HT + ((size_t)(tok >> 4) * 48 * 64 + (tok & 15) + 16 * hh) * 16 + (size_t)(2 * (2 * nt + ch)) * 1024;
        const float k1 = -1.4426950408889634f * rs;
        const f32x2 k12 = {k1, k1};
        const float irs2 = __builtin_amdgcn_rcpf(rs * rs);
        const f32x2 ir2 = {irs2, irs2};
#pragma unroll
        for (int c = 0; c < 2; ++c) {
          unsigned P[8];
#pragma unroll
          for (int j = 0; j < 8; ++j) {
            f32x2 gg = {acc[g][c][2 * j], acc[g][c][2 * j + 1]};
            f32x2 uu = {acc[g][2 + c][2 * j], acc[g][2 + c][2 * j + 1]};
            f32x2 t = gg * k12;
            f32x2 e = {__builtin_amdgcn_exp2f(t[0]), __builtin_amdgcn_exp2f(t[1])};
            f32x2 d = e * ir2 + ir2;
            f32x2 rr = {__builtin_amdgcn_rcpf(d[0]), __builtin_amdgcn_rcpf(d[1])};
            f32x2 hv = (gg * uu) * rr;
            P[j] = pk2(hv[0], hv[1]);
          }
#pragma unroll
          for (int qp = 0; qp < 2; ++qp) {
            auto r0 = __builtin_amdgcn_permlane32_swap(P[4 * qp], P[4 * qp + 2], false, false);
            auto r1 = __builtin_amdgcn_permlane32_swap(P[4 * qp + 1], P[4 * qp + 3], false, false);
            u32x4 v = {r0[0], r1[0], r0[1], r1[1]};
            st_nt(ht + c * 1024 + qp * 512, v);
          }
        }
      } else {
#pragma unroll
        for (int c5 = 0; c5 < 4; ++c5) store_qk(acc[g][c5], qcol0 + 128 * ch + 32 * c5);
      }
    }
  } else {
    int voff[5];
    float cn[5];
#pragma unroll
    for (int c5 = 0; c5 < 5; ++c5) {
      const int nv = qcol0 - 1280 + ((c5 < 4) ? (128 * ch + 32 * c5) : (256 + 32 * ch)) + r;
      const int head = nv / 20, d = nv - head * 20;
      voff[c5] = head * VT_HEAD + hh * 336 + d * 16;
      cn[c5] = csb[c5 * 32 + r];
    }
#pragma unroll
    for (int g = 0; g < 2; ++g) {
      const int tb = 32 * (2 * tw + (g ^ ch));
      f32x2 mm[8], rr[8];
#pragma unroll
      for (int q4 = 0; q4 < 4; ++q4) {
        f32x4 a = *(LAS f32x4*)(lds + K1_ST + (tb + 8 * q4 + 4 * hh) * 8);
        f32x4 b = *(LAS f32x4*)(lds + K1_ST + (tb + 8 * q4 + 4 * hh) * 8 + 16);
        mm[2 * q4] = f32x2{a[0], a[2]}; rr[2 * q4] = f32x2{a[1], a[3]};
        mm[2 * q4 + 1] = f32x2{b[0], b[2]}; rr[2 * q4 + 1] = f32x2{b[1], b[3]};
      }
      char* vb = ws + OFF_VT + ((rb * 128 + tb) >> 4) * VT_TG;
#pragma unroll
      for (int c5 = 0; c5 < 5; ++c5) {
        const f32x2 c2 = {cn[c5], cn[c5]};
#pragma unroll
        for (int s = 0; s < 2; ++s) {
          u32x4 v;
#pragma unroll
          for (int j = 0; j < 4; ++j) {
            f32x2 t = {acc[g][c5][8 * s + 2 * j], acc[g][c5][8 * s + 2 * j + 1]};
            t = (mm[4 * s + j] * c2 + t) * rr[4 * s + j];
            v[j] = pk2(t[0], t[1]);
          }
          st_nt(vb + voff[c5] + s * VT_TG, v);
        }
      }
    }
  }
#undef lane
#undef r
#undef hh
}

__global__ __launch_bounds__(512, 2) void k1_kernel(const char* __restrict__ x, const char* __restrict__ wqkv,
                                                    const char* __restrict__ wo, const char* __restrict__ wg,
                                                    const char* __restrict__ wu, const char* __restrict__ wd,
                                                    char* __restrict__ ws) {
  __shared__ __attribute__((aligned(16))) char smem[K1_LDS];
  LAS char* lds = (LAS char*)smem;
  const int b = blockIdx.x, xcd = b & 7, idx = b >> 3;
  const int rb = 8 * (xcd & 1) + (idx & 7), nt = 4 * (xcd >> 1) + (idx >> 3);
  if (b == 0 && threadIdx.x < 8) *(unsigned*)(ws + OFF_CNT + threadIdx.x * 256) = 0u;
  if (nt < 12) k1_body<0>(x, wqkv, wg, wu, wd, wo, ws, lds, rb, nt);
  else if (nt < 14) k1_body<1>(x, wqkv, wg, wu, wd, wo, ws, lds, rb, nt);
  else k1_body<2>(x, wqkv, wg, wu, wd, wo, ws, lds, rb, nt);
}

#define GSLEEP 8
constexpr int K2_K = 0;
constexpr int K2_Q = 24576;
constexpr int K2_V = 36864;
constexpr int K2_FLAG = K2_V + 21504;
constexpr int K2_RED = K2_FLAG + 64;
constexpr int RED_ROW = 336;
constexpr int K2_LDS = K2_RED + 4 * 64 * RED_ROW;

__global__ __launch_bounds__(512, 2) void k2_kernel(const char* __restrict__ x, char* __restrict__ ws,
                                                    char* __restrict__ out) {
  __shared__ __attribute__((aligned(16))) char smem[K2_LDS];
  LAS char* lds = (LAS char*)smem;
  const int tid = threadIdx.x, lane = tid & 63;
  const int w = __builtin_amdgcn_readfirstlane(tid >> 6);
  const int b = blockIdx.x, xcd = b & 7, idx = b >> 3;
  LAS unsigned* flg = (LAS unsigned*)(lds + K2_FLAG);
  if (tid < 4) flg[tid] = 0u;
  asm volatile("s_waitcnt lgkmcnt(0)\n\ts_barrier" ::: "memory");

  if (w < 4) {
    const int head = 4 * xcd + (idx & 3), rb = idx >> 2;
    const int r = lane & 31, hh = lane >> 5;
    {
      const char* kq = ws + OFF_KQ + lane * 16;
      for (int j = w; j < 12; j += 4) {
        const int ck = j >> 2, qc = j & 3;
        glds16(kq + (size_t)(head * 3 + ck) * 32768 + (256 * rb + 64 * qc) * 16, lds + K2_Q + j * 1024);
      }
      for (int j = w; j < 24; j += 4) {
        const int ck = j >> 3, qc = j & 7;
        if (rb > 0 || qc >= 4)
          glds16(kq + (size_t)((32 + head) * 3 + ck) * 32768 + (256 * rb - 256 + 64 * qc) * 16, lds + K2_K + j * 1024);
        else
          *(LAS u32x4*)(lds + K2_K + j * 1024 + lane * 16) = u32x4{0u, 0u, 0u, 0u};
      }
      const char* vt = ws + OFF_VT + (size_t)head * VT_HEAD + (16 * rb - 16) * VT_TG + lane * 16;
      for (int j = w; j < 21; j += 4) {
        if (rb > 0 || j >= 11 || (j == 10 && lane >= 32)) glds16(vt + j * 1024, lds + K2_V + j * 1024);
        else *(LAS u32x4*)(lds + K2_V + j * 1024 + lane * 16) = u32x4{0u, 0u, 0u, 0u};
      }
      if (rb == 0) asm volatile("s_waitcnt vmcnt(0) lgkmcnt(0)" ::: "memory");
      else if (w == 0) asm volatile("s_waitcnt vmcnt(6)" ::: "memory");
      else asm volatile("s_waitcnt vmcnt(5)" ::: "memory");
      if (lane == 0) __hip_atomic_fetch_add(flg, 1u, __ATOMIC_RELAXED, __HIP_MEMORY_SCOPE_WORKGROUP);
    }
    f32x16 mk0, mk8;
#pragma unroll
    for (int j = 0; j < 16; ++j) {
      const int kl = (j & 3) + 8 * (j >> 2) + 4 * hh;
      mk0[j] = (kl > r) ? 0.f : -INFINITY;
      mk8[j] = (kl <= r) ? 0.f : -INFINITY;
    }
    for (int sp = 0; sp < (1 << 22); ++sp) {
      if (__hip_atomic_load(flg, __ATOMIC_RELAXED, __HIP_MEMORY_SCOPE_WORKGROUP) >= 4u) break;
      __builtin_amdgcn_s_sleep(1);
    }
    asm volatile("" ::: "memory");
    const int dcl = (r < 20) ? r : 20;
    bf16x8 q0[2], q1[2];
    float mrow[2];
    f32x16 z;
#pragma unroll
    for (int j = 0; j < 16; ++j) z[j] = 0.f;
#pragma unroll
    for (int qi = 0; qi < 2; ++qi) {
      const int qt = 2 * w + qi;
      const int tmin = (rb == 0) ? (8 - qt) : 0;
      {
        LAS char* qp = lds + K2_Q + (32 * qt + r) * 16;
        q0[qi] = *(LAS bf16x8*)(qp + hh * 4096);
        u32x2 c = *(LAS u32x2*)(qp + 2 * 4096);
        u32x4 t1_ = {hh ? 0u : c[0], hh ? 0u : c[1], 0u, 0u};
        q1[qi] = __builtin_bit_cast(bf16x8, t1_);
      }
      LAS char* kp = lds + K2_K + (32 * qt + r) * 16;
      LAS char* kpa = kp + hh * 8192;
      bf16x8 ka[3], kc[3];
      auto rdk = [&](int t) {
        ka[t % 3] = *(LAS bf16x8*)(kpa + t * 512);
        u32x2 c_ = *(LAS u32x2*)(kp + 2 * 8192 + t * 512);
        kc[t % 3] = __builtin_bit_cast(bf16x8, u32x4{c_[0], c_[1], 0u, 0u});
      };
      rdk(8); rdk(7);
      float mx[4];
      f32x16 sa, sb;
      __builtin_amdgcn_sched_barrier(0);
#pragma unroll
      for (int t = 8; t >= -1; --t) {
        if (t >= 0) {
          f32x16 c0 = (t == 8) ? mk8 : ((t == 0) ? mk0 : z);
          f32x16 v = MFMA32(ka[t % 3], q0[qi], c0);
          if (t - 2 >= 0) rdk(t - 2);
          v = MFMA32(kc[t % 3], q1[qi], v);
          if (t & 1) sb = v; else sa = v;
        }
        if (t < 8) {
          const f32x16& p = ((t + 1) & 1) ? sb : sa;
          if (t + 1 == 8) {
#pragma unroll
            for (int u = 0; u < 4; ++u) mx[u] = fmaxf(fmaxf(p[4 * u], p[4 * u + 1]), fmaxf(p[4 * u + 2], p[4 * u + 3]));
          } else if (t + 1 >= tmin) {
#pragma unroll
            for (int j = 0; j < 16; j += 8)
#pragma unroll
              for (int u = 0; u < 4; ++u) mx[u] = fmaxf(fmaxf(mx[u], p[j + 2 * u]), p[j + 2 * u + 1]);
          }
          asm volatile("" : "+v"(mx[0]), "+v"(mx[1]), "+v"(mx[2]), "+v"(mx[3]));
        }
        __builtin_amdgcn_sched_barrier(0);
      }
      mrow[qi] = swap_max(fmaxf(fmaxf(mx[0], mx[1]), fmaxf(mx[2], mx[3])));
    }
    asm volatile("s_waitcnt vmcnt(0) lgkmcnt(0)" ::: "memory");
    {
      const int o1 = (lane >> 1) * VT_TG + (lane & 1) * 336 + 320;
      if (((o1 >> 10) & 3) == w) {
        u32x4 one = {0x3f803f80u, 0x3f803f80u, 0x3f803f80u, 0x3f803f80u};
        *(LAS u32x4*)(lds + K2_V + o1) = one;
      }
      asm volatile("s_waitcnt lgkmcnt(0)" ::: "memory");
    }
    if (lane == 0) __hip_atomic_fetch_add(flg + 2, 1u, __ATOMIC_RELAXED, __HIP_MEMORY_SCOPE_WORKGROUP);
    for (int sp = 0; sp < (1 << 22); ++sp) {
      if (__hip_atomic_load(flg + 2, __ATOMIC_RELAXED, __HIP_MEMORY_SCOPE_WORKGROUP) >= 4u) break;
      __builtin_amdgcn_s_sleep(1);
    }
    asm volatile("" ::: "memory");
#pragma unroll 1
    for (int qi = 0; qi < 2; ++qi) {
      const int qt = 2 * w + qi;
      const int tmin = (rb == 0) ? (8 - qt) : 0;
      const bf16x8 qa = qi ? q0[1] : q0[0], qb = qi ? q1[1] : q1[0];
      const float nm = -(qi ? mrow[1] : mrow[0]);
      f32x16 cm, c0m, c8m;
#pragma unroll
      for (int j = 0; j < 16; ++j) { cm[j] = nm; c0m[j] = mk0[j] + nm; c8m[j] = mk8[j] + nm; }
      LAS char* kp = lds + K2_K + (32 * qt + r) * 16;
      LAS char* kpa = kp + hh * 8192;
      LAS char* vp = lds + K2_V + (2 * qt) * VT_TG + dcl * 16 + hh * 336;
      bf16x8 ka[3], kc[3], vf[3][2];
      auto rdk = [&](int t) {
        ka[t % 3] = *(LAS bf16x8*)(kpa + t * 512);
        u32x2 c_ = *(LAS u32x2*)(kp + 2 * 8192 + t * 512);
        kc[t % 3] = __builtin_bit_cast(bf16x8, u32x4{c_[0], c_[1], 0u, 0u});
      };
      auto rdv = [&](int t) {
        vf[t % 3][0] = *(LAS bf16x8*)(vp + (2 * t) * VT_TG);
        vf[t % 3][1] = *(LAS bf16x8*)(vp + (2 * t + 1) * VT_TG);
      };
      rdk(8); rdk(7); rdv(8);
      f32x16 o, sa, sb;
#pragma unroll
      for (int j = 0; j < 16; ++j) o[j] = 0.f;
      u32x4 pp[2];
      __builtin_amdgcn_sched_barrier(0);
#pragma unroll
      for (int t = 8; t >= -2; --t) {
        if (t >= 0) {
          f32x16 c0 = (t == 8) ? c8m : ((t == 0) ? c0m : cm);
          f32x16 v = MFMA32(ka[t % 3], qa, c0);
          v = MFMA32(kc[t % 3], qb, v);
          if (t & 1) sb = v; else sa = v;
          if (t - 2 >= 0) rdk(t - 2);
        }
        if (t + 2 <= 8) {
          if (t + 2 >= tmin) {
            o = MFMA32(vf[(t + 2) % 3][0], __builtin_bit_cast(bf16x8, pp[0]), o);
            o = MFMA32(vf[(t + 2) % 3][1], __builtin_bit_cast(bf16x8, pp[1]), o);
          }
        }
        if (t - 1 >= 0) rdv(t - 1);
        __builtin_amdgcn_sched_barrier(0);
        if (t + 1 <= 8 && t + 1 >= 0) {
          f32x16& p = ((t + 1) & 1) ? sb : sa;
#pragma unroll
          for (int j = 0; j < 16; ++j) p[j] = __builtin_amdgcn_exp2f(p[j]);
          asm volatile("" : "+v"(p));
          __builtin_amdgcn_sched_barrier(0);
#pragma unroll
          for (int s = 0; s < 2; ++s)
#pragma unroll
            for (int j = 0; j < 4; ++j) pp[s][j] = pk2(p[8 * s + 2 * j], p[8 * s + 2 * j + 1]);
          asm volatile("" : "+v"(pp[0]), "+v"(pp[1]));
        }
        __builtin_amdgcn_sched_barrier(0);
      }
      const float rl = __builtin_amdgcn_rcpf(o[12]);
      const int tok = 256 * rb + 32 * qt + r;
      char* ob = ws + OFF_OT + ((size_t)(tok >> 4) * 20 * 64 + (tok & 15)) * 16;
#pragma unroll
      for (int a = 0; a < 3; ++a) {
        const int k = head * 20 + 8 * a + 4 * hh;
        unsigned long long v = (unsigned long long)pk2(o[4 * a] * rl, o[4 * a + 1] * rl) |
                               ((unsigned long long)pk2(o[4 * a + 2] * rl, o[4 * a + 3] * rl) << 32);
        char* p = ob + (size_t)(k >> 5) * 1024 + ((k >> 3) & 3) * 256 + (k & 7) * 2;
        if (a < 2 || hh == 0)
          __hip_atomic_store((unsigned long long*)p, v, __ATOMIC_RELAXED, __HIP_MEMORY_SCOPE_AGENT);
      }
    }
    asm volatile("s_waitcnt vmcnt(0)" ::: "memory");
    if (lane == 0) {
      unsigned old = __hip_atomic_fetch_add(flg + 1, 1u, __ATOMIC_RELAXED, __HIP_MEMORY_SCOPE_WORKGROUP);
      if (old == 3u)
        __hip_atomic_fetch_add((unsigned*)(ws + OFF_CNT + rb * 256), 1u, __ATOMIC_RELAXED, __HIP_MEMORY_SCOPE_AGENT);
    }
    return;
  }

  const int kw = w - 4;
  const int rb64 = 8 * (xcd >> 1) + (idx & 7), ct = 4 * (xcd & 1) + (idx >> 3);
  const char* wp = ws + OFF_W2 + (size_t)ct * 68 * 5 * 1024 + lane * 16;
  const char* hp = ws + OFF_HT + (size_t)(4 * rb64) * 48 * 1024 + lane * 16;
  auto orsrc = __builtin_amdgcn_make_buffer_rsrc((void*)(ws + OFF_OT), 0, (int)SZ_OT, 0x00020000);
  const int ovoff = (4 * rb64) * 20 * 1024 + lane * 16;
  f32x4 acc[5][4];
#pragma unroll
  for (int n = 0; n < 5; ++n)
#pragma unroll
    for (int i = 0; i < 4; ++i) acc[n][i] = f32x4{0.f, 0.f, 0.f, 0.f};
  constexpr int GD = 4;
  bf16x8 bw[GD][5], bt[GD][4];
  auto loadT = [&](int st, int sl) {
    if (st < 12) {
      const int ks = 12 * kw + st;
#pragma unroll
      for (int i = 0; i < 4; ++i) bt[sl][i] = *(const bf16x8*)(hp + (size_t)(i * 48 + ks) * 1024);
    } else {
      const int ko = 5 * kw + (st - 12);
#pragma unroll
      for (int i = 0; i < 4; ++i)
        bt[sl][i] = __builtin_bit_cast(bf16x8, __builtin_amdgcn_raw_buffer_load_b128(orsrc, ovoff + (i * 20 + ko) * 1024, 0, 16));
    }
    __builtin_amdgcn_sched_barrier(0);
  };
  auto loadW = [&](int st, int sl) {
    const int ks = (st < 12) ? (12 * kw + st) : (48 + 5 * kw + (st - 12));
#pragma unroll
    for (int n = 0; n < 5; ++n) bw[sl][n] = *(const bf16x8*)(wp + (size_t)(ks * 5 + n) * 1024);
    __builtin_amdgcn_sched_barrier(0);
  };
  auto mm = [&](int st) {
#pragma unroll
    for (int n = 0; n < 5; ++n)
#pragma unroll
      for (int i = 0; i < 4; ++i) acc[n][i] = MFMA16(bw[st % GD][n], bt[st % GD][i], acc[n][i]);
    __builtin_amdgcn_sched_barrier(0);
  };
  __builtin_amdgcn_s_sleep(GSLEEP);
#pragma unroll
  for (int st = 0; st < GD; ++st) { loadT(st, st); loadW(st, st); }
#pragma unroll
  for (int st = 0; st < 12; ++st) {
    mm(st);
    if (st + GD < 12) loadT(st + GD, st % GD);
    loadW(st + GD, st % GD);
  }
  const int tq = tid - 256;
  u32x4 xv[3];
#pragma unroll
  for (int it = 0; it < 3; ++it) {
    const int item = tq + 256 * it;
    const int tk = item / 10, n8 = item - tk * 10;
    if (item < 640) xv[it] = *(const u32x4*)(x + (size_t)(64 * rb64 + tk) * 1280 + (80 * ct + 8 * n8) * 2);
  }
  {
    const unsigned* cnt = (const unsigned*)(ws + OFF_CNT + (rb64 >> 2) * 256);
    for (int sp = 0; sp < (1 << 22); ++sp) {
      if (__hip_atomic_load(cnt, __ATOMIC_RELAXED, __HIP_MEMORY_SCOPE_AGENT) >= 32u) break;
      __builtin_amdgcn_s_sleep(1);
    }
    __builtin_amdgcn_fence(__ATOMIC_ACQUIRE, "wavefront");
    asm volatile("" ::: "memory");
  }
#pragma unroll
  for (int st = 12; st < 16; ++st) loadT(st, st % GD);
  mm(12);
  loadT(16, 0); loadW(16, 0);
  mm(13); mm(14); mm(15); mm(16);
  {
    LAS char* rp = lds + K2_RED + (kw * 64 + (lane & 15)) * RED_ROW + (lane >> 4) * 16;
#pragma unroll
    for (int n = 0; n < 5; ++n)
#pragma unroll
      for (int i = 0; i < 4; ++i) *(LAS f32x4*)(rp + i * 16 * RED_ROW + n * 64) = acc[n][i];
  }
  asm volatile("s_waitcnt lgkmcnt(0)\n\ts_barrier" ::: "memory");
#pragma unroll
  for (int it = 0; it < 3; ++it) {
    const int item = tq + 256 * it;
    const int tk = item / 10, n8 = item - tk * 10;
    if (item < 640) {
      LAS char* rp = lds + K2_RED + tk * RED_ROW + n8 * 32;
      f32x4 a = *(LAS f32x4*)rp, c = *(LAS f32x4*)(rp + 16);
#pragma unroll
      for (int k = 1; k < 4; ++k) {
        a += *(LAS f32x4*)(rp + k * 64 * RED_ROW);
        c += *(LAS f32x4*)(rp + k * 64 * RED_ROW + 16);
      }
      u32x4 xx = xv[it];
      u32x4 v;
      v[0] = pk2(a[0] + __uint_as_float(xx[0] << 16), a[1] + __uint_as_float(xx[0] & 0xffff0000u));
      v[1] = pk2(a[2] + __uint_as_float(xx[1] << 16), a[3] + __uint_as_float(xx[1] & 0xffff0000u));
      v[2] = pk2(c[0] + __uint_as_float(xx[2] << 16), c[1] + __uint_as_float(xx[2] & 0xffff0000u));
      v[3] = pk2(c[2] + __uint_as_float(xx[3] << 16), c[3] + __uint_as_float(xx[3] & 0xffff0000u));
      *(u32x4*)(out + (size_t)(64 * rb64 + tk) * 1280 + (80 * ct + 8 * n8) * 2) = v;
    }
  }
}

}

extern "C" void kernel_launch(void* const* d_in, const int* in_sizes, int n_in, void* d_out, int out_size,
                              void* d_ws, size_t ws_size, hipStream_t stream) {
  const char* x = (const char*)d_in[0];
  const char* wqkv = (const char*)d_in[1];
  const char* wo = (const char*)d_in[2];
  const char* wg = (const char*)d_in[4];
  const char* wu = (const char*)d_in[5];
  const char* wd = (const char*)d_in[6];
  k1_kernel<<<256, 512, 0, stream>>>(x, wqkv, wo, wg, wu, wd, (char*)d_ws);
  k2_kernel<<<256, 512, 0, stream>>>(x, (char*)d_ws, (char*)d_out);
}
```

```cpp
#include <hip/hip_runtime.h>
#include <hip/hip_bf16.h>
#include <stdint.h>

typedef short bf16x8 __attribute__((ext_vector_type(8)));
typedef short s16x4 __attribute__((ext_vector_type(4)));
typedef float f32x16 __attribute__((ext_vector_type(16)));
typedef float f32x4 __attribute__((ext_vector_type(4)));
typedef float f32x2 __attribute__((ext_vector_type(2)));
typedef unsigned u32x4 __attribute__((ext_vector_type(4)));
typedef unsigned u32x2 __attribute__((ext_vector_type(2)));
typedef __bf16 bf2 __attribute__((ext_vector_type(2)));
#define PFD 10
#define NO_PF
#define STAUX 0
#define STAUX8 0
#define LAS __attribute__((address_space(3)))
#define DI __device__ __forceinline__

namespace {

constexpr int S_ = 2048, D_ = 640, F_ = 1536;
constexpr float QSCALE = 0.22360679774997896f * 1.4426950408889634f;

constexpr size_t OFF_CNT = 0;
constexpr size_t OFF_KQ = 32768;
constexpr size_t SZ_KQ = 160ull * 2048 * 16;
constexpr size_t OFF_VT = OFF_KQ + SZ_KQ;
constexpr int VT_TG = 21 * 32;
constexpr int VT_HEAD = 128 * VT_TG;
constexpr size_t SZ_VT = 32ull * VT_HEAD;
constexpr size_t OFF_HT = OFF_VT + SZ_VT;
constexpr size_t SZ_HT = 128ull * 48 * 1024;
constexpr size_t OFF_OT = OFF_HT + SZ_HT;
constexpr size_t SZ_OT = 128ull * 20 * 1024;
constexpr size_t OFF_W2 = OFF_OT + SZ_OT;

DI float swap_sum(float x) {
  auto r = __builtin_amdgcn_permlane32_swap(__float_as_uint(x), __float_as_uint(x), false, false);
  return __uint_as_float(r[0]) + __uint_as_float(r[1]);
}
DI float swap_max(float x) {
  auto r = __builtin_amdgcn_permlane32_swap(__float_as_uint(x), __float_as_uint(x), false, false);
  return fmaxf(__uint_as_float(r[0]), __uint_as_float(r[1]));
}
DI unsigned pk2(float a, float b) {
  f32x2 v = {a, b};
  return __builtin_bit_cast(unsigned, __builtin_convertvector(v, bf2));
}
DI float dot2(unsigned a, unsigned b, float c) {
  return __builtin_amdgcn_fdot2_f32_bf16(__builtin_bit_cast(bf2, a), __builtin_bit_cast(bf2, b), c, false);
}
DI void glds16(const char* g, LAS char* l) {
  __builtin_amdgcn_global_load_lds((const __attribute__((address_space(1))) void*)g, (LAS void*)l, 16, 0, 0);
}
DI s16x4 tr_read(LAS char* p) {
  return __builtin_amdgcn_ds_read_tr16_b64_v4i16((LAS s16x4*)p);
}
template <int N> DI void wait_bar() {
  asm volatile("s_waitcnt vmcnt(%0) lgkmcnt(0)\n\ts_barrier" ::"i"(N) : "memory");
}
template <int N> DI void wait_bar_nl() {
  asm volatile("s_waitcnt vmcnt(%0)\n\ts_barrier" ::"i"(N) : "memory");
}
#define MFMA32(a, b, c) __builtin_amdgcn_mfma_f32_32x32x16_bf16((a), (b), (c), 0, 0, 0)
#define MFMA16(a, b, c) __builtin_amdgcn_mfma_f32_16x16x32_bf16((a), (b), (c), 0, 0, 0)

constexpr int K1_WSLOT = 20480, K1_NWS = 5;
constexpr int K1_XOFF = K1_WSLOT * K1_NWS;
constexpr int K1_XSLOT = 16384, K1_NXS = 3;
constexpr int K1_CS = K1_XOFF + K1_XSLOT * K1_NXS;
constexpr int K1_ST = K1_CS + 2560;
constexpr int K1_LDS = K1_ST + 1024;

DI void retile_load(const char* __restrict__ wd, const char* __restrict__ wo, int item, u32x4 (&in)[8]) {
  const int G = item / 80, ng = item - G * 80;
  const char* src = (G < 192) ? (wd + (size_t)(8 * G) * 1280) : (wo + (size_t)(8 * G - 1536) * 1280);
  src += ng * 16;
#pragma unroll
  for (int i = 0; i < 8; ++i) in[i] = *(const u32x4*)(src + i * 1280);
}
DI void retile_store(char* __restrict__ ws, int item, const u32x4 (&in)[8]) {
  const int G = item / 80, ng = item - G * 80;
  const int n = 8 * ng, ct = n / 80, nn = n - ct * 80;
  char* dst = ws + OFF_W2 + ((((size_t)ct * 68 + (G >> 2)) * 5 + (nn >> 4)) * 64 + (nn & 15) + 16 * (G & 3)) * 16;
#pragma unroll
  for (int a = 0; a < 4; ++a) {
    u32x4 lo, hi;
#pragma unroll
    for (int b = 0; b < 4; ++b) {
      lo[b] = __builtin_amdgcn_perm(in[2 * b + 1][a], in[2 * b][a], 0x05040100u);
      hi[b] = __builtin_amdgcn_perm(in[2 * b + 1][a], in[2 * b][a], 0x07060302u);
    }
    *(u32x4*)(dst + (2 * a) * 16) = lo;
    *(u32x4*)(dst + (2 * a + 1) * 16) = hi;
  }
}
DI float bf_lo(unsigned v) { return __uint_as_float(v << 16); }
DI float bf_hi(unsigned v) { return __uint_as_float(v & 0xffff0000u); }
DI void bar_only() { asm volatile("s_barrier" ::: "memory"); }

DI void st_ws(__amdgpu_buffer_rsrc_t rs, unsigned off, u32x4 v) { __builtin_amdgcn_raw_buffer_store_b128(v, rs, (int)off, 0, STAUX); }
DI void st_ws(__amdgpu_buffer_rsrc_t rs, unsigned off, u32x2 v) { __builtin_amdgcn_raw_buffer_store_b64(v, rs, (int)off, 0, STAUX8); }

template <int TYPE>
DI void k1_body(const char* __restrict__ x, const char* __restrict__ wqkv, const char* __restrict__ wg,
                const char* __restrict__ wu, const char* __restrict__ wd, const char* __restrict__ wo,
                char* __restrict__ ws, LAS char* lds, int rb, int nt) {
  constexpr int NU = (TYPE == 1) ? 4 : 5;
  const int tid = threadIdx.x, lane = tid & 63;
  const int w = __builtin_amdgcn_readfirstlane(tid >> 6);
  const int qcol0 = (TYPE == 0) ? nt * 64 : (TYPE == 1) ? (12 + 4 * (nt - 12)) * 64 : 1280 + 5 * (nt - 14) * 64;

  if (w >= 4) {
    const int hw = w - 4;
    const char* ub[5];
    if (TYPE == 0) {
      ub[0] = wg + (2 * nt) * 128; ub[1] = wu + (2 * nt) * 128;
      ub[2] = wg + (2 * nt + 1) * 128; ub[3] = wu + (2 * nt + 1) * 128;
      ub[4] = wqkv + qcol0 * 2;
    } else {
      for (int u = 0; u < 5; ++u) ub[u] = wqkv + (qcol0 + 64 * u) * 2;
    }
    unsigned voffA, voffB, xvoff0, xvoff1;
    {
      int row = 8 * hw + (lane >> 3);
      int cc = ((((lane >> 2) & 1) ^ ((row >> 1) & 1)) << 2) + (lane & 3);
      voffA = row * 3072 + cc * 16;
      voffB = row * 3840 + cc * 16;
      int xr = rb * 128 + 32 * hw + (lane >> 3);
      int sc = (lane & 7) ^ (lane >> 4);
      xvoff0 = xr * 1280 + sc * 16;
      xvoff1 = xr * 1280 + (sc ^ 4) * 16;
    }
    const unsigned ldsb = (unsigned)(size_t)lds;
    auto dma = [&](const char* sbase, unsigned voff, unsigned dst) {
      asm volatile("s_mov_b32 m0, %0\n\ts_nop 0\n\tglobal_load_lds_dwordx4 %1, %2" ::"s"(dst), "v"(voff), "s"(sbase) : "memory", "m0");
    };
    auto issueW = [&](int slot) {
      const unsigned dst = ldsb + (slot % K1_NWS) * 4096 + hw * 1024;
#pragma unroll
      for (int u = 0; u < NU; ++u) {
        const bool a = (TYPE == 0 && u < 4);
        dma(ub[u] + (size_t)slot * 32 * (a ? 3072 : 3840), a ? voffA : voffB, dst + u * K1_WSLOT);
      }
    };
    auto issueX = [&](int c) {
      const unsigned dst = ldsb + K1_XOFF + (c % K1_NXS) * K1_XSLOT + hw * 4096;
#pragma unroll
      for (int i = 0; i < 4; ++i) dma(x + c * 128 + i * 8 * 1280, (i & 1) ? xvoff1 : xvoff0, dst + i * 1024);
    };
    issueW(0); issueX(0); issueW(1); issueW(2); issueX(1);
    wait_bar<2 * NU + 4>();
    issueW(3);
#pragma unroll
    for (int i = 0; i < 19; ++i) {
      const int nw = ((i + 3 < 19) ? (i + 3) : 19) - (i + 1);
      const bool hx = (i & 1) ? (i <= 15) : (i <= 16);
      const int cnt = NU * nw + (hx ? 4 : 0);
      if (cnt == 2 * NU + 4) wait_bar<2 * NU + 4>();
      else if (cnt == 2 * NU) wait_bar<2 * NU>();
      else if (cnt == NU) wait_bar<NU>();
      else wait_bar<0>();
      if (i + 4 < 20) issueW(i + 4);
      if ((i & 1) == 0 && (i / 2 + 2) < 10) issueX(i / 2 + 2);
    }
    bar_only();
    {
      const int xcd = blockIdx.x & 7, ks = (blockIdx.x >> 3) + 32 * hw;
      if (hw < 3 && ks < 68) {
        const char* src = (ks < 48) ? (wd + (size_t)(32 * ks) * 1280) : (wo + (size_t)(32 * (ks - 48)) * 1280);
        src += xcd * 160;
        const unsigned scr = hw * 5120;
#pragma unroll
        for (int j = 0; j < 5; ++j) {
          const int sl = j * 64 + lane, row = sl / 10, cc = sl - row * 10;
          dma(src, row * 1280 + cc * 16, ldsb + scr + j * 1024);
        }
        asm volatile("s_waitcnt vmcnt(0)" ::: "memory");
        LAS char* tp = lds + scr + (8 * (lane >> 4) + ((lane & 15) >> 2)) * 160 + 8 * (lane & 3);
        char* dst = ws + OFF_W2 + (size_t)((xcd * 68 + ks) * 5) * 1024 + lane * 16;
#pragma unroll
        for (int n = 0; n < 5; ++n) {
          s16x4 lo = tr_read(tp + n * 32), hi = tr_read(tp + n * 32 + 640);
          *(bf16x8*)(dst + n * 1024) = __builtin_shufflevector(lo, hi, 0, 1, 2, 3, 4, 5, 6, 7);
        }
      }
    }
    return;
  }

  const int tw = w & 1, ch = w >> 1;
  const int r = lane & 31, hh = lane >> 5;
  int xa[2][2];
  {
    int e = hh ^ ((r >> 1) & 7);
#pragma unroll
    for (int g = 0; g < 2; ++g)
#pragma unroll
      for (int ph = 0; ph < 2; ++ph) {
        int tg = 2 * tw + (g ^ ch), s = ph ^ tw;
        xa[g][ph] = K1_XOFF + (32 * tg + r) * 128 + (((2 * s) ^ e) << 4);
      }
  }
  int wa[2][3];
  {
    int G = lane >> 4, chh = G & 1, q = (lane & 15) >> 2, p = lane & 3;
#pragma unroll
    for (int ph = 0; ph < 2; ++ph) {
      int b0 = (8 * hh + q) * 128 + 32 * chh + 8 * p + (ph ^ tw) * 2048;
      wa[ph][0] = b0 + 64 * (0 ^ (q >> 1)) + ch * 2 * K1_WSLOT;
      wa[ph][1] = b0 + 64 * (1 ^ (q >> 1)) + ch * 2 * K1_WSLOT;
      wa[ph][2] = b0 + 64 * (ch ^ (q >> 1)) + 4 * K1_WSLOT;
    }
  }
  f32x16 acc[2][5];
#pragma unroll
  for (int g = 0; g < 2; ++g)
#pragma unroll
    for (int i = 0; i < 5; ++i)
#pragma unroll
      for (int j = 0; j < 16; ++j) acc[g][i][j] = 0.f;
  f32x4 cs[5];
#pragma unroll
  for (int i = 0; i < 5; ++i) cs[i] = f32x4{0.f, 0.f, 0.f, 0.f};
  bf16x8 onep;
  {
    const unsigned o = ((lane & 15) == ((lane >> 4) & 1)) ? 0x3f803f80u : 0u;
    u32x4 t = {o, o, o, o};
    onep = __builtin_bit_cast(bf16x8, t);
  }
  float s0 = 0.f, q0 = 0.f;

  bf16x8 xf[2];
  s16x4 wl[5], wh[5];
  auto rdx = [&](int i, int ph, int g) {
    LAS char* xb = lds + ((i / 2) % K1_NXS) * K1_XSLOT;
    xf[g] = *(LAS bf16x8*)(xb + (xa[g][ph] ^ ((i & 1) * 64)));
  };
  auto rdw = [&](int i, int ph, int c5) {
    LAS char* p = lds + (i % K1_NWS) * 4096 + ((c5 < 4) ? ((c5 >> 1) * K1_WSLOT + wa[ph][c5 & 1]) : wa[ph][2]);
    wl[c5] = tr_read(p);
    wh[c5] = tr_read(p + 512);
  };
  auto phase = [&](int i, int ph, int ni, int nph, bool do_rd) {
#pragma unroll
    for (int k = 0; k < 2 * NU; ++k) {
      const int g = (k >= NU) ? 1 : 0, c5 = k - g * NU;
      bf16x8 wfr = __builtin_shufflevector(wl[c5], wh[c5], 0, 1, 2, 3, 4, 5, 6, 7);
      if (TYPE == 2) acc[g][c5] = MFMA32(xf[g], wfr, acc[g][c5]);
      else acc[g][c5] = MFMA32(wfr, xf[g], acc[g][c5]);
      if (ph == 0 && g == 0) cs[c5] = MFMA16(wfr, onep, cs[c5]);
      if (k == 0) rdx(i, ph, 1);
      if (do_rd && g == 1) {
        if (c5 == 0) rdx(ni, nph, 0);
        rdw(ni, nph, c5);
      }
      if (k < 4) {
        u32x4 xu = __builtin_bit_cast(u32x4, xf[0]);
        float l = bf_lo(xu[k]), h = bf_hi(xu[k]);
        s0 += l; q0 = fmaf(l, l, q0); s0 += h; q0 = fmaf(h, h, q0);
        asm volatile("" : "+v"(s0), "+v"(q0));
      }
      __builtin_amdgcn_sched_barrier(0);
    }
  };

  unsigned pfd = 0, pfoff;
  const char* pfb;
  int pfstep;
  {
    const int rbi = rb & 7, nti = nt & 3;
    if (w == 3) {
      pfb = x + (size_t)(rb * 128 + 32 * nti) * 1280; pfoff = (lane & 31) * 1280; pfstep = 64;
    } else if (TYPE == 0) {
      if (w == 2) { pfb = wqkv + qcol0 * 2; pfoff = (4 * rbi + (lane & 3)) * 3840; pfstep = 32 * 3840; }
      else { pfb = (w ? wu : wg) + (2 * nt) * 128; pfoff = (4 * rbi + (lane & 3)) * 3072 + ((lane >> 2) & 1) * 128; pfstep = 32 * 3072; }
    } else {
      int u = w * 2 + ((lane >> 2) & 1); if (u >= NU) u = NU - 1;
      pfb = wqkv + qcol0 * 2; pfoff = (4 * rbi + (lane & 3)) * 3840 + u * 128; pfstep = 32 * 3840;
    }
  }
  auto pf = [&](int slot) {
  };
#pragma unroll
  for (int s_ = 4; s_ < PFD; ++s_) pf(s_);
  bar_only();
  rdx(0, 0, 0);
#pragma unroll
  for (int c5 = 0; c5 < NU; ++c5) rdw(0, 0, c5);
  __builtin_amdgcn_sched_barrier(0);
#pragma unroll
  for (int i = 0; i < 20; ++i) {
    phase(i, 0, i, 1, true);
    if (i < 19) {
      bar_only();
      if (i + PFD < 20) pf(i + PFD);
      if (i == 18) asm volatile("s_waitcnt vmcnt(0)" : "+v"(pfd) :: "memory");
      __builtin_amdgcn_sched_barrier(0);
      phase(i, 1, i + 1, 0, true);
    } else {
      phase(i, 1, 0, 0, false);
    }
  }

  int tz = threadIdx.x;
  asm volatile("" : "+v"(tz));
  const int lane_ = tz & 63, r_ = tz & 31, hh_ = (tz >> 5) & 1;
#define lane lane_
#define r r_
#define hh hh_
  LAS float* csb = (LAS float*)(lds + K1_CS) + ch * 160;
  const __amdgpu_buffer_rsrc_t wsr = __builtin_amdgcn_make_buffer_rsrc(ws, 0, 0x7fffffff, 0x00020000);
  {
    const float ssum = swap_sum(s0), ssq = swap_sum(q0);
    const float mean = ssum * (1.f / 640.f);
    const float var = fmaxf(ssq * (1.f / 640.f) - mean * mean, 0.f);
    const float rs = __builtin_amdgcn_rsqf(var + 1e-5f);
    if (lane < 32) {
      f32x2 st = {-mean, rs};
      *(LAS f32x2*)(lds + K1_ST + (32 * (2 * tw + ch) + lane) * 8) = st;
    }
    if ((lane & 15) < 2) {
      LAS float* cp = csb + tw * 320 + 16 * (lane & 15) + 4 * (lane >> 4);
#pragma unroll
      for (int c5 = 0; c5 < NU; ++c5) *(LAS f32x4*)(cp + c5 * 32) = cs[c5];
    }
  }
  asm volatile("s_waitcnt lgkmcnt(0)\n\ts_barrier" ::: "memory");

  if (TYPE != 2) {
    f32x2 st[2];
#pragma unroll
    for (int g = 0; g < 2; ++g) st[g] = *(LAS f32x2*)(lds + K1_ST + (32 * (2 * tw + (g ^ ch)) + r) * 8);
#pragma unroll
    for (int c5 = 0; c5 < NU; ++c5) {
      f32x4 c4[4];
#pragma unroll
      for (int q4 = 0; q4 < 4; ++q4) c4[q4] = *(LAS f32x4*)(csb + c5 * 32 + 8 * q4 + 4 * hh) + *(LAS f32x4*)(csb + 320 + c5 * 32 + 8 * q4 + 4 * hh);
#pragma unroll
      for (int g = 0; g < 2; ++g) {
        const f32x2 nm2 = {st[g][0], st[g][0]};
#pragma unroll
        for (int q4 = 0; q4 < 4; ++q4)
#pragma unroll
          for (int j = 0; j < 2; ++j) {
            f32x2 v = {acc[g][c5][4 * q4 + 2 * j], acc[g][c5][4 * q4 + 2 * j + 1]};
            v = nm2 * f32x2{c4[q4][2 * j], c4[q4][2 * j + 1]} + v;
            acc[g][c5][4 * q4 + 2 * j] = v[0]; acc[g][c5][4 * q4 + 2 * j + 1] = v[1];
          }
      }
    }
#pragma unroll
    for (int g = 0; g < 2; ++g) {
      const int tok = rb * 128 + 32 * (2 * tw + (g ^ ch)) + r;
      const float rs = st[g][1];
      auto store_qk = [&](const f32x16& a, int nb) {
        const unsigned kq = (unsigned)OFF_KQ + tok * 16 + (nb / 8 + hh) * 32768;
        const float rsq = (nb < 640) ? rs * QSCALE : rs;
        const f32x2 rs2 = {rsq, rsq};
        unsigned P[8];
#pragma unroll
        for (int j = 0; j < 8; ++j) {
          f32x2 v = f32x2{a[2 * j], a[2 * j + 1]} * rs2;
          P[j] = pk2(v[0], v[1]);
        }
#pragma unroll
        for (int qp = 0; qp < 2; ++qp) {
          auto r0 = __builtin_amdgcn_permlane32_swap(P[4 * qp], P[4 * qp + 2], false, false);
          auto r1 = __builtin_amdgcn_permlane32_swap(P[4 * qp + 1], P[4 * qp + 3], false, false);
          u32x4 v = {r0[0], r1[0], r0[1], r1[1]};
          st_ws(wsr, kq + qp * 65536, v);
        }
      };
      if (TYPE == 0) {
        store_qk(acc[g][4], qcol0 + 32 * ch);
        const unsigned ht = (unsigned)OFF_HT + ((tok >> 4) * 48 * 64 + (tok & 15) + 16 * hh) * 16 + (2 * (2 * nt + ch)) * 1024;
        const float k1 = -1.4426950408889634f * rs;
        const f32x2 k12 = {k1, k1};
        const float irs2 = __builtin_amdgcn_rcpf(rs * rs);
        const f32x2 ir2 = {irs2, irs2};
#pragma unroll
        for (int c = 0; c < 2; ++c) {
          unsigned P[8];
#pragma unroll
          for (int j = 0; j < 8; ++j) {
            f32x2 gg = {acc[g][c][2 * j], acc[g][c][2 * j + 1]};
            f32x2 uu = {acc[g][2 + c][2 * j], acc[g][2 + c][2 * j + 1]};
            f32x2 t = gg * k12;
            f32x2 e = {__builtin_amdgcn_exp2f(t[0]), __builtin_amdgcn_exp2f(t[1])};
            f32x2 d = e * ir2 + ir2;
            f32x2 rr = {__builtin_amdgcn_rcpf(d[0]), __builtin_amdgcn_rcpf(d[1])};
            f32x2 hv = (gg * uu) * rr;
            P[j] = pk2(hv[0], hv[1]);
          }
#pragma unroll
          for (int qp = 0; qp < 2; ++qp) {
            auto r0 = __builtin_amdgcn_permlane32_swap(P[4 * qp], P[4 * qp + 2], false, false);
            auto r1 = __builtin_amdgcn_permlane32_swap(P[4 * qp + 1], P[4 * qp + 3], false, false);
            u32x4 v = {r0[0], r1[0], r0[1], r1[1]};
            st_ws(wsr, ht + c * 1024 + qp * 512, v);
          }
        }
      } else {
#pragma unroll
        for (int c5 = 0; c5 < 4; ++c5) store_qk(acc[g][c5], qcol0 + 128 * ch + 32 * c5);
      }
    }
  } else {
    int voff[5];
    float cn[5];
#pragma unroll
    for (int c5 = 0; c5 < 5; ++c5) {
      const int nv = qcol0 - 1280 + ((c5 < 4) ? (128 * ch + 32 * c5) : (256 + 32 * ch)) + r;
      const int head = nv / 20, d = nv - head * 20;
      voff[c5] = head * VT_HEAD + hh * 336 + d * 16;
      cn[c5] = csb[c5 * 32 + r] + csb[320 + c5 * 32 + r];
    }
#pragma unroll
    for (int g = 0; g < 2; ++g) {
      const int tb = 32 * (2 * tw + (g ^ ch));
      f32x2 mm[8], rr[8];
#pragma unroll
      for (int q4 = 0; q4 < 4; ++q4) {
        f32x4 a = *(LAS f32x4*)(lds + K1_ST + (tb + 8 * q4 + 4 * hh) * 8);
        f32x4 b = *(LAS f32x4*)(lds + K1_ST + (tb + 8 * q4 + 4 * hh) * 8 + 16);
        mm[2 * q4] = f32x2{a[0], a[2]}; rr[2 * q4] = f32x2{a[1], a[3]};
        mm[2 * q4 + 1] = f32x2{b[0], b[2]}; rr[2 * q4 + 1] = f32x2{b[1], b[3]};
      }
      const unsigned vb = (unsigned)OFF_VT + ((rb * 128 + tb) >> 4) * VT_TG;
#pragma unroll
      for (int c5 = 0; c5 < 5; ++c5) {
        const f32x2 c2 = {cn[c5], cn[c5]};
#pragma unroll
        for (int s = 0; s < 2; ++s) {
          u32x4 v;
#pragma unroll
          for (int j = 0; j < 4; ++j) {
            f32x2 t = {acc[g][c5][8 * s + 2 * j], acc[g][c5][8 * s + 2 * j + 1]};
            t = (mm[4 * s + j] * c2 + t) * rr[4 * s + j];
            v[j] = pk2(t[0], t[1]);
          }
          st_ws(wsr, vb + voff[c5] + s * VT_TG, v);
        }
      }
    }
  }
#undef lane
#undef r
#undef hh
}

__global__ __launch_bounds__(512, 2) void k1_kernel(const char* __restrict__ x, const char* __restrict__ wqkv,
                                                    const char* __restrict__ wo, const char* __restrict__ wg,
                                                    const char* __restrict__ wu, const char* __restrict__ wd,
                                                    char* __restrict__ ws) {
  __shared__ __attribute__((aligned(16))) char smem[K1_LDS];
  LAS char* lds = (LAS char*)smem;
  const int b = blockIdx.x, xcd = b & 7, idx = b >> 3;
  const int rb = 8 * (xcd & 1) + (idx & 7), nt = 4 * (xcd >> 1) + (idx >> 3);
  if (b == 0 && threadIdx.x < 8) *(unsigned*)(ws + OFF_CNT + threadIdx.x * 256) = 0u;
  if (nt < 12) k1_body<0>(x, wqkv, wg, wu, wd, wo, ws, lds, rb, nt);
  else if (nt < 14) k1_body<1>(x, wqkv, wg, wu, wd, wo, ws, lds, rb, nt);
  else k1_body<2>(x, wqkv, wg, wu, wd, wo, ws, lds, rb, nt);
}

#define PSLEEP 2
#define GSLEEP 8
constexpr int K2_K = 0;
constexpr int K2_Q = 24576;
constexpr int K2_V = 36864;
constexpr int K2_FLAG = K2_V + 21504;
constexpr int K2_RED = K2_FLAG + 64;
constexpr int RED_ROW = 336;
constexpr int K2_LDS = K2_RED + 4 * 64 * RED_ROW;

__global__ __launch_bounds__(768, 3) void k2_kernel(const char* __restrict__ x, char* __restrict__ ws,
                                                    char* __restrict__ out) {
  __shared__ __attribute__((aligned(16))) char smem[K2_LDS];
  LAS char* lds = (LAS char*)smem;
  const int tid = threadIdx.x, lane = tid & 63;
  const int w = __builtin_amdgcn_readfirstlane(tid >> 6);
  const int b = blockIdx.x, xcd = b & 7, idx = b >> 3;
  LAS unsigned* flg = (LAS unsigned*)(lds + K2_FLAG);
  if (tid < 8) flg[tid] = 0u;
  asm volatile("s_waitcnt lgkmcnt(0)\n\ts_barrier" ::: "memory");

  if (w < 8) {
    const int head = 4 * xcd + (idx & 3), rb = idx >> 2;
    const int r = lane & 31, hh = lane >> 5;
    {
      const unsigned l16 = lane * 16;
      const unsigned ldsb = (unsigned)(size_t)lds;
      auto dma = [&](const char* sbase, unsigned dst) {
        asm volatile("s_mov_b32 m0, %0\n\ts_nop 0\n\tglobal_load_lds_dwordx4 %1, %2" ::"s"(dst), "v"(l16), "s"(sbase) : "memory", "m0");
      };
      const char* qb_ = ws + OFF_KQ + (size_t)((head * 5) >> 1) * 32768 + rb * 4096 + (w & 3) * 1024;
      dma(qb_ + (w >> 2) * 32768, ldsb + K2_Q + w * 1024);
      dma(qb_ + 2 * 32768, ldsb + K2_Q + (8 + (w & 3)) * 1024);
      const char* kb_ = ws + OFF_KQ + (size_t)(80 + ((head * 5) >> 1)) * 32768 + rb * 4096 - 4096 + w * 1024;
      dma(kb_, ldsb + K2_K + w * 1024);
      dma(kb_ + 32768, ldsb + K2_K + (8 + w) * 1024);
      dma(kb_ + 65536, ldsb + K2_K + (16 + w) * 1024);
      const char* vb_ = ws + OFF_VT + (size_t)head * VT_HEAD + (16 * rb - 16) * VT_TG;
      const int j3 = (w + 16 <= 20) ? (w + 16) : w;
      dma(vb_ + w * 1024, ldsb + K2_V + w * 1024);
      dma(vb_ + (w + 8) * 1024, ldsb + K2_V + (w + 8) * 1024);
      dma(vb_ + j3 * 1024, ldsb + K2_V + j3 * 1024);
    }
    f32x16 mk0, mk8;
#pragma unroll
    for (int j = 0; j < 16; ++j) {
      const int kl = (j & 3) + 8 * (j >> 2) + 4 * hh;
      mk0[j] = (kl > r) ? 0.f : -INFINITY;
      mk8[j] = (kl <= r) ? 0.f : -INFINITY;
    }
    asm volatile("" : "+v"(mk0), "+v"(mk8));
    asm volatile("s_waitcnt vmcnt(3)" ::: "memory");
    if (lane == 0) __hip_atomic_fetch_add(flg, 1u, __ATOMIC_RELAXED, __HIP_MEMORY_SCOPE_WORKGROUP);
    for (int sp = 0; sp < (1 << 22); ++sp) {
      if (__hip_atomic_load(flg, __ATOMIC_RELAXED, __HIP_MEMORY_SCOPE_WORKGROUP) >= 8u) break;
      __builtin_amdgcn_s_sleep(1);
    }
    asm volatile("" ::: "memory");
    const int dcl = (r < 20) ? r : 20;
    const int qt = w;
    const int tmin = (rb == 0) ? (8 - qt) : 0;
    bf16x8 qa, qb;
    {
      LAS char* qp = lds + K2_Q + (32 * qt + r) * 16;
      qa = *(LAS bf16x8*)(qp + (hh + (head & 1)) * 4096);
      u32x2 c = *(LAS u32x2*)(qp + ((head & 1) ? 8 : 2 * 4096));
      u32x4 t1_ = {hh ? 0u : c[0], hh ? 0u : c[1], 0u, 0u};
      qb = __builtin_bit_cast(bf16x8, t1_);
    }
    LAS char* kp = lds + K2_K + (32 * qt + r) * 16;
    LAS char* kpa = kp + (hh + (head & 1)) * 8192;
    LAS char* kpc = kp + ((head & 1) ? 8 : 2 * 8192);
    bf16x8 ka[3], kc[3], vf[3][2];
    auto rdk = [&](int t) {
      ka[t % 3] = *(LAS bf16x8*)(kpa + t * 512);
      u32x2 c_ = *(LAS u32x2*)(kpc + t * 512);
      kc[t % 3] = __builtin_bit_cast(bf16x8, u32x4{c_[0], c_[1], 0u, 0u});
    };
    f32x16 sa, sb;
    float nm;
    {
      f32x16 z;
#pragma unroll
      for (int j = 0; j < 16; ++j) z[j] = 0.f;
      rdk(8); rdk(7);
      float mx[4];
      __builtin_amdgcn_sched_barrier(0);
#pragma unroll
      for (int t = 8; t >= -1; --t) {
        if (t >= 0) {
          f32x16 c0 = (t == 8) ? mk8 : ((t == 0) ? mk0 : z);
          f32x16 v = MFMA32(ka[t % 3], qa, c0);
          if (t - 2 >= 0) rdk(t - 2);
          v = MFMA32(kc[t % 3], qb, v);
          if (t & 1) sb = v; else sa = v;
        }
        if (t < 8) {
          const f32x16& p = ((t + 1) & 1) ? sb : sa;
          if (t + 1 == 8) {
#pragma unroll
            for (int u = 0; u < 4; ++u) mx[u] = fmaxf(fmaxf(p[4 * u], p[4 * u + 1]), fmaxf(p[4 * u + 2], p[4 * u + 3]));
          } else if (t + 1 >= tmin) {
#pragma unroll
            for (int j = 0; j < 16; j += 8)
#pragma unroll
              for (int u = 0; u < 4; ++u) mx[u] = fmaxf(fmaxf(mx[u], p[j + 2 * u]), p[j + 2 * u + 1]);
          }
          asm volatile("" : "+v"(mx[0]), "+v"(mx[1]), "+v"(mx[2]), "+v"(mx[3]));
        }
        __builtin_amdgcn_sched_barrier(0);
      }
      nm = -swap_max(fmaxf(fmaxf(mx[0], mx[1]), fmaxf(mx[2], mx[3])));
    }
    asm volatile("s_waitcnt vmcnt(0) lgkmcnt(0)" ::: "memory");
    {
      const int o1 = (lane >> 1) * VT_TG + (lane & 1) * 336 + 320;
      if (((o1 >> 10) & 7) == w) {
        u32x4 one = {0x3f803f80u, 0x3f803f80u, 0x3f803f80u, 0x3f803f80u};
        *(LAS u32x4*)(lds + K2_V + o1) = one;
      }
      asm volatile("s_waitcnt lgkmcnt(0)" ::: "memory");
    }
    if (lane == 0) __hip_atomic_fetch_add(flg + 2, 1u, __ATOMIC_RELAXED, __HIP_MEMORY_SCOPE_WORKGROUP);
    rdk(8); rdk(7);
    f32x16 cm;
#pragma unroll
    for (int j = 0; j < 16; ++j) { cm[j] = nm; mk0[j] += nm; mk8[j] += nm; }
    for (int sp = 0; sp < (1 << 22); ++sp) {
      if (__hip_atomic_load(flg + 2, __ATOMIC_RELAXED, __HIP_MEMORY_SCOPE_WORKGROUP) >= 8u) break;
      __builtin_amdgcn_s_sleep(1);
    }
    asm volatile("" ::: "memory");
    {
      LAS char* vp = lds + K2_V + (2 * qt) * VT_TG + dcl * 16 + hh * 336;
      auto rdv = [&](int t) {
        vf[t % 3][0] = *(LAS bf16x8*)(vp + (2 * t) * VT_TG);
        vf[t % 3][1] = *(LAS bf16x8*)(vp + (2 * t + 1) * VT_TG);
      };
      rdv(8);
      f32x16 o;
#pragma unroll
      for (int j = 0; j < 16; ++j) o[j] = 0.f;
      u32x4 pp[2];
      __builtin_amdgcn_sched_barrier(0);
#pragma unroll
      for (int t = 8; t >= -2; --t) {
        const bool ex = (t + 1 <= 8 && t + 1 >= 0);
        f32x16& p = ((t + 1) & 1) ? sb : sa;
        f32x16& v = (t & 1) ? sb : sa;
        auto exps = [&](int k) {
          if (ex) {
#pragma unroll
            for (int j = 4 * k; j < 4 * k + 4; ++j) p[j] = __builtin_amdgcn_exp2f(p[j]);
            asm volatile("" : "+v"(p));
          }
          __builtin_amdgcn_sched_barrier(0);
        };
        const bool pv = (t + 2 <= 8) && (t + 2 >= tmin);
        if (t + 2 <= 8) { if (pv) o = MFMA32(vf[(t + 2) % 3][0], __builtin_bit_cast(bf16x8, pp[0]), o); }
        exps(0);
        if (t + 2 <= 8) { if (pv) o = MFMA32(vf[(t + 2) % 3][1], __builtin_bit_cast(bf16x8, pp[1]), o); }
        if (t - 1 >= 0) rdv(t - 1);
        exps(1);
        if (t >= 0) {
          f32x16 c0 = (t == 8) ? mk8 : ((t == 0) ? mk0 : cm);
          v = MFMA32(ka[t % 3], qa, c0);
        }
        exps(2);
        if (t >= 0) {
          v = MFMA32(kc[t % 3], qb, v);
          if (t - 2 >= 0) rdk(t - 2);
        }
        exps(3);
        if (ex) {
#pragma unroll
          for (int s = 0; s < 2; ++s)
#pragma unroll
            for (int j = 0; j < 4; ++j) pp[s][j] = pk2(p[8 * s + 2 * j], p[8 * s + 2 * j + 1]);
          asm volatile("" : "+v"(pp[0]), "+v"(pp[1]));
        }
        __builtin_amdgcn_sched_barrier(0);
      }
      const float rl = __builtin_amdgcn_rcpf(o[12]);
      const int tok = 256 * rb + 32 * qt + r;
      char* ob = ws + OFF_OT + ((size_t)(tok >> 4) * 20 * 64 + (tok & 15)) * 16;
#pragma unroll
      for (int a = 0; a < 3; ++a) {
        const int k = head * 20 + 8 * a + 4 * hh;
        unsigned long long v = (unsigned long long)pk2(o[4 * a] * rl, o[4 * a + 1] * rl) |
                               ((unsigned long long)pk2(o[4 * a + 2] * rl, o[4 * a + 3] * rl) << 32);
        char* p = ob + (size_t)(k >> 5) * 1024 + ((k >> 3) & 3) * 256 + (k & 7) * 2;
        if (a < 2 || hh == 0)
          __hip_atomic_store((unsigned long long*)p, v, __ATOMIC_RELAXED, __HIP_MEMORY_SCOPE_AGENT);
      }
    }
    asm volatile("s_waitcnt vmcnt(0)" ::: "memory");
    if (lane == 0) {
      unsigned old = __hip_atomic_fetch_add(flg + 1, 1u, __ATOMIC_RELAXED, __HIP_MEMORY_SCOPE_WORKGROUP);
      if (old == 7u)
        __hip_atomic_fetch_add((unsigned*)(ws + OFF_CNT + rb * 256), 1u, __ATOMIC_RELAXED, __HIP_MEMORY_SCOPE_AGENT);
    }
    return;
  }

  const int kw = w - 8;
  const int rb64 = 8 * (xcd >> 1) + (idx & 7), ct = 4 * (xcd & 1) + (idx >> 3);
  const char* wp = ws + OFF_W2 + (size_t)ct * 68 * 5 * 1024 + lane * 16;
  const char* hp = ws + OFF_HT + (size_t)(4 * rb64) * 48 * 1024 + lane * 16;
  auto orsrc = __builtin_amdgcn_make_buffer_rsrc((void*)(ws + OFF_OT), 0, (int)SZ_OT, 0x00020000);
  const int ovoff = (4 * rb64) * 20 * 1024 + lane * 16;
  f32x4 acc[5][4];
#pragma unroll
  for (int n = 0; n < 5; ++n)
#pragma unroll
    for (int i = 0; i < 4; ++i) acc[n][i] = f32x4{0.f, 0.f, 0.f, 0.f};
  constexpr int GD = 2;
  bf16x8 bw[GD][5], bt[GD][4];
  auto loadT = [&](int st, int sl) {
    if (st < 12) {
      const int ks = 12 * kw + st;
#pragma unroll
      for (int i = 0; i < 4; ++i) bt[sl][i] = *(const bf16x8*)(hp + (size_t)(i * 48 + ks) * 1024);
    } else {
      const int ko = 5 * kw + (st - 12);
#pragma unroll
      for (int i = 0; i < 4; ++i)
        bt[sl][i] = __builtin_bit_cast(bf16x8, __builtin_amdgcn_raw_buffer_load_b128(orsrc, ovoff + (i * 20 + ko) * 1024, 0, 16));
    }
    __builtin_amdgcn_sched_barrier(0);
  };
  auto loadW = [&](int st, int sl) {
    const int ks = (st < 12) ? (12 * kw + st) : (48 + 5 * kw + (st - 12));
#pragma unroll
    for (int n = 0; n < 5; ++n) bw[sl][n] = *(const bf16x8*)(wp + (size_t)(ks * 5 + n) * 1024);
    __builtin_amdgcn_sched_barrier(0);
  };
  auto mm = [&](int st) {
#pragma unroll
    for (int n = 0; n < 5; ++n)
#pragma unroll
      for (int i = 0; i < 4; ++i) acc[n][i] = MFMA16(bw[st % GD][n], bt[st % GD][i], acc[n][i]);
    __builtin_amdgcn_sched_barrier(0);
  };
  __builtin_amdgcn_s_sleep(GSLEEP);
#pragma unroll
  for (int st = 0; st < GD; ++st) { loadT(st, st); loadW(st, st); }
#pragma unroll
  for (int st = 0; st < 12; ++st) {
    mm(st);
    if (st + GD < 12) loadT(st + GD, st % GD);
    loadW(st + GD, st % GD);
  }
  {
    if (kw == 0) {
      const unsigned* cnt = (const unsigned*)(ws + OFF_CNT + (rb64 >> 2) * 256);
      for (int sp = 0; sp < (1 << 22); ++sp) {
        if (__hip_atomic_load(cnt, __ATOMIC_RELAXED, __HIP_MEMORY_SCOPE_AGENT) >= 32u) break;
        __builtin_amdgcn_s_sleep(PSLEEP);
      }
      __hip_atomic_store(flg + 3, 1u, __ATOMIC_RELAXED, __HIP_MEMORY_SCOPE_WORKGROUP);
    } else {
      for (int sp = 0; sp < (1 << 24); ++sp) {
        if (__hip_atomic_load(flg + 3, __ATOMIC_RELAXED, __HIP_MEMORY_SCOPE_WORKGROUP) != 0u) break;
        __builtin_amdgcn_s_sleep(1);
      }
    }
    __builtin_amdgcn_fence(__ATOMIC_ACQUIRE, "wavefront");
    asm volatile("" ::: "memory");
  }
  loadT(12, 0); loadT(13, 1);
  mm(12); loadT(14, 0); loadW(14, 0);
  mm(13); loadT(15, 1); loadW(15, 1);
  mm(14); loadT(16, 0); loadW(16, 0);
  mm(15);
  mm(16);
  const int tq = tid - 512;
  u32x4 xv[3];
#pragma unroll
  for (int it = 0; it < 3; ++it) {
    const int item = tq + 256 * it;
    const int tk = item / 10, n8 = item - tk * 10;
    if (item < 640) xv[it] = *(const u32x4*)(x + (size_t)(64 * rb64 + tk) * 1280 + (80 * ct + 8 * n8) * 2);
  }
  {
    LAS char* rp = lds + K2_RED + (kw * 64 + (lane & 15)) * RED_ROW + (lane >> 4) * 16;
#pragma unroll
    for (int n = 0; n < 5; ++n)
#pragma unroll
      for (int i = 0; i < 4; ++i) *(LAS f32x4*)(rp + i * 16 * RED_ROW + n * 64) = acc[n][i];
  }
  asm volatile("s_waitcnt lgkmcnt(0)\n\ts_barrier" ::: "memory");
#pragma unroll
  for (int it = 0; it < 3; ++it) {
    const int item = tq + 256 * it;
    const int tk = item / 10, n8 = item - tk * 10;
    if (item < 640) {
      LAS char* rp = lds + K2_RED + tk * RED_ROW + n8 * 32;
      f32x4 a = *(LAS f32x4*)rp, c = *(LAS f32x4*)(rp + 16);
#pragma unroll
      for (int k = 1; k < 4; ++k) {
        a += *(LAS f32x4*)(rp + k * 64 * RED_ROW);
        c += *(LAS f32x4*)(rp + k * 64 * RED_ROW + 16);
      }
      u32x4 xx = xv[it];
      u32x4 v;
      v[0] = pk2(a[0] + __uint_as_float(xx[0] << 16), a[1] + __uint_as_float(xx[0] & 0xffff0000u));
      v[1] = pk2(a[2] + __uint_as_float(xx[1] << 16), a[3] + __uint_as_float(xx[1] & 0xffff0000u));
      v[2] = pk2(c[0] + __uint_as_float(xx[2] << 16), c[1] + __uint_as_float(xx[2] & 0xffff0000u));
      v[3] = pk2(c[2] + __uint_as_float(xx[3] << 16), c[3] + __uint_as_float(xx[3] & 0xffff0000u));
      *(u32x4*)(out + (size_t)(64 * rb64 + tk) * 1280 + (80 * ct + 8 * n8) * 2) = v;
    }
  }
}

}

extern "C" void kernel_launch(void* const* d_in, const int* in_sizes, int n_in, void* d_out, int out_size,
                              void* d_ws, size_t ws_size, hipStream_t stream) {
  const char* x = (const char*)d_in[0];
  const char* wqkv = (const char*)d_in[1];
  const char* wo = (const char*)d_in[2];
  const char* wg = (const char*)d_in[4];
  const char* wu = (const char*)d_in[5];
  const char* wd = (const char*)d_in[6];
  k1_kernel<<<256, 512, 0, stream>>>(x, wqkv, wo, wg, wu, wd, (char*)d_ws);
  k2_kernel<<<256, 768, 0, stream>>>(x, (char*)d_ws, (char*)d_out);
}
```

```cpp
#include <hip/hip_runtime.h>
#include <hip/hip_bf16.h>
#include <stdint.h>

typedef short bf16x8 __attribute__((ext_vector_type(8)));
typedef short s16x4 __attribute__((ext_vector_type(4)));
typedef float f32x16 __attribute__((ext_vector_type(16)));
typedef float f32x4 __attribute__((ext_vector_type(4)));
typedef float f32x2 __attribute__((ext_vector_type(2)));
typedef unsigned u32x4 __attribute__((ext_vector_type(4)));
typedef unsigned u32x2 __attribute__((ext_vector_type(2)));
typedef __bf16 bf2 __attribute__((ext_vector_type(2)));
#define PFD 10
#define NO_PF
#define STAUX 0
#define STAUX8 0
#define LAS __attribute__((address_space(3)))
#define DI __device__ __forceinline__

namespace {

constexpr int S_ = 2048, D_ = 640, F_ = 1536;
constexpr float QSCALE = 0.22360679774997896f * 1.4426950408889634f;

constexpr size_t OFF_CNT = 0;
constexpr size_t OFF_KQ = 32768;
constexpr size_t SZ_KQ = 160ull * 2048 * 16;
constexpr size_t OFF_VT = OFF_KQ + SZ_KQ;
constexpr int VT_TG = 21 * 32;
constexpr int VT_HEAD = 128 * VT_TG;
constexpr size_t SZ_VT = 32ull * VT_HEAD;
constexpr size_t OFF_HT = OFF_VT + SZ_VT;
constexpr size_t SZ_HT = 128ull * 48 * 1024;
constexpr size_t OFF_OT = OFF_HT + SZ_HT;
constexpr size_t SZ_OT = 128ull * 20 * 1024;
constexpr size_t OFF_W2 = OFF_OT + SZ_OT;

DI float swap_sum(float x) {
  auto r = __builtin_amdgcn_permlane32_swap(__float_as_uint(x), __float_as_uint(x), false, false);
  return __uint_as_float(r[0]) + __uint_as_float(r[1]);
}
DI float swap_max(float x) {
  auto r = __builtin_amdgcn_permlane32_swap(__float_as_uint(x), __float_as_uint(x), false, false);
  return fmaxf(__uint_as_float(r[0]), __uint_as_float(r[1]));
}
DI unsigned pk2(float a, float b) {
  f32x2 v = {a, b};
  return __builtin_bit_cast(unsigned, __builtin_convertvector(v, bf2));
}
DI float dot2(unsigned a, unsigned b, float c) {
  return __builtin_amdgcn_fdot2_f32_bf16(__builtin_bit_cast(bf2, a), __builtin_bit_cast(bf2, b), c, false);
}
DI void glds16(const char* g, LAS char* l) {
  __builtin_amdgcn_global_load_lds((const __attribute__((address_space(1))) void*)g, (LAS void*)l, 16, 0, 0);
}
DI s16x4 tr_read(LAS char* p) {
  return __builtin_amdgcn_ds_read_tr16_b64_v4i16((LAS s16x4*)p);
}
template <int N> DI void wait_bar() {
  asm volatile("s_waitcnt vmcnt(%0) lgkmcnt(0)\n\ts_barrier" ::"i"(N) : "memory");
}
template <int N> DI void wait_bar_nl() {
  asm volatile("s_waitcnt vmcnt(%0)\n\ts_barrier" ::"i"(N) : "memory");
}
#define MFMA32(a, b, c) __builtin_amdgcn_mfma_f32_32x32x16_bf16((a), (b), (c), 0, 0, 0)
#define MFMA16(a, b, c) __builtin_amdgcn_mfma_f32_16x16x32_bf16((a), (b), (c), 0, 0, 0)

constexpr int K1_WSLOT = 16384, K1_NWS = 4;
constexpr int K1_XOFF = K1_WSLOT * 5;
constexpr int K1_XSLOT = 16384, K1_NXS = 2;
constexpr int K1_CS = K1_XOFF + K1_XSLOT * K1_NXS;
constexpr int K1_ST = K1_CS + 2560;
constexpr int K1_LDS = K1_ST + 1024;

DI void retile_load(const char* __restrict__ wd, const char* __restrict__ wo, int item, u32x4 (&in)[8]) {
  const int G = item / 80, ng = item - G * 80;
  const char* src = (G < 192) ? (wd + (size_t)(8 * G) * 1280) : (wo + (size_t)(8 * G - 1536) * 1280);
  src += ng * 16;
#pragma unroll
  for (int i = 0; i < 8; ++i) in[i] = *(const u32x4*)(src + i * 1280);
}
DI void retile_store(char* __restrict__ ws, int item, const u32x4 (&in)[8]) {
  const int G = item / 80, ng = item - G * 80;
  const int n = 8 * ng, ct = n / 80, nn = n - ct * 80;
  char* dst = ws + OFF_W2 + ((((size_t)ct * 68 + (G >> 2)) * 5 + (nn >> 4)) * 64 + (nn & 15) + 16 * (G & 3)) * 16;
#pragma unroll
  for (int a = 0; a < 4; ++a) {
    u32x4 lo, hi;
#pragma unroll
    for (int b = 0; b < 4; ++b) {
      lo[b] = __builtin_amdgcn_perm(in[2 * b + 1][a], in[2 * b][a], 0x05040100u);
      hi[b] = __builtin_amdgcn_perm(in[2 * b + 1][a], in[2 * b][a], 0x07060302u);
    }
    *(u32x4*)(dst + (2 * a) * 16) = lo;
    *(u32x4*)(dst + (2 * a + 1) * 16) = hi;
  }
}
DI float bf_lo(unsigned v) { return __uint_as_float(v << 16); }
DI float bf_hi(unsigned v) { return __uint_as_float(v & 0xffff0000u); }
DI void bar_only() { asm volatile("s_barrier" ::: "memory"); }

DI void st_ws(__amdgpu_buffer_rsrc_t rs, unsigned off, u32x4 v) { __builtin_amdgcn_raw_buffer_store_b128(v, rs, (int)off, 0, STAUX); }
DI void st_ws(__amdgpu_buffer_rsrc_t rs, unsigned off, u32x2 v) { __builtin_amdgcn_raw_buffer_store_b64(v, rs, (int)off, 0, STAUX8); }

template <int TYPE>
DI void k1_body(const char* __restrict__ x, const char* __restrict__ wqkv, const char* __restrict__ wg,
                const char* __restrict__ wu, const char* __restrict__ wd, const char* __restrict__ wo,
                char* __restrict__ ws, LAS char* lds, int rb, int nt) {
  constexpr int NU = (TYPE == 1) ? 4 : 5;
  const int tid = threadIdx.x, lane = tid & 63;
  const int w = __builtin_amdgcn_readfirstlane(tid >> 6);
  const int qcol0 = (TYPE == 0) ? nt * 64 : (TYPE == 1) ? (12 + 4 * (nt - 12)) * 64 : 1280 + 5 * (nt - 14) * 64;

  if (w >= 4) {
    const int hw = w - 4;
    const char* ub[5];
    if (TYPE == 0) {
      ub[0] = wg + (2 * nt) * 128; ub[1] = wu + (2 * nt) * 128;
      ub[2] = wg + (2 * nt + 1) * 128; ub[3] = wu + (2 * nt + 1) * 128;
      ub[4] = wqkv + qcol0 * 2;
    } else {
      for (int u = 0; u < 5; ++u) ub[u] = wqkv + (qcol0 + 64 * u) * 2;
    }
    unsigned voffA, voffB, xvoff0, xvoff1;
    {
      int row = 8 * hw + (lane >> 3);
      int cc = ((((lane >> 2) & 1) ^ ((row >> 1) & 1)) << 2) + (lane & 3);
      voffA = row * 3072 + cc * 16;
      voffB = row * 3840 + cc * 16;
      int xr = rb * 128 + 32 * hw + (lane >> 3);
      int sc = (lane & 7) ^ (lane >> 4);
      xvoff0 = xr * 1280 + sc * 16;
      xvoff1 = xr * 1280 + (sc ^ 4) * 16;
    }
    const unsigned ldsb = (unsigned)(size_t)lds;
    auto dma = [&](const char* sbase, unsigned voff, unsigned dst) {
      asm volatile("s_mov_b32 m0, %0\n\ts_nop 0\n\tglobal_load_lds_dwordx4 %1, %2" ::"s"(dst), "v"(voff), "s"(sbase) : "memory", "m0");
    };
    auto issueW = [&](int slot, int s4) {
      const unsigned dst = ldsb + s4 * 4096 + hw * 1024;
#pragma unroll
      for (int u = 0; u < NU; ++u) {
        const bool a = (TYPE == 0 && u < 4);
        dma(ub[u] + (size_t)slot * 32 * (a ? 3072 : 3840), a ? voffA : voffB, dst + u * K1_WSLOT);
      }
    };
    auto issueX = [&](int c, int c2) {
      const unsigned dst = ldsb + K1_XOFF + c2 * K1_XSLOT + hw * 4096;
#pragma unroll
      for (int i = 0; i < 4; ++i) dma(x + c * 128 + i * 8 * 1280, (i & 1) ? xvoff1 : xvoff0, dst + i * 1024);
    };
    issueW(0, 0); issueX(0, 0); issueW(1, 1); issueX(1, 1);
    wait_bar<NU + 4>();
    issueW(2, 2);
#pragma unroll 1
    for (int it = 0; it < 5; ++it) {
#pragma unroll
      for (int s4 = 0; s4 < 4; ++s4) {
        const int i = it * 4 + s4;
        if (it < 4) {
          if (s4 & 1) wait_bar<NU>();
          else wait_bar<NU + 4>();
        } else wait_bar<0>();
        if (i + 3 < 20) issueW(i + 3, (s4 + 3) & 3);
        if ((s4 & 1) && (i + 3) / 2 < 10) issueX((i + 3) / 2, ((s4 + 3) / 2) & 1);
      }
    }
    bar_only();
    {
      const int xcd = blockIdx.x & 7, ks = (blockIdx.x >> 3) + 32 * hw;
      if (hw < 3 && ks < 68) {
        const char* src = (ks < 48) ? (wd + (size_t)(32 * ks) * 1280) : (wo + (size_t)(32 * (ks - 48)) * 1280);
        src += xcd * 160;
        const unsigned scr = hw * 5120;
#pragma unroll
        for (int j = 0; j < 5; ++j) {
          const int sl = j * 64 + lane, row = sl / 10, cc = sl - row * 10;
          dma(src, row * 1280 + cc * 16, ldsb + scr + j * 1024);
        }
        asm volatile("s_waitcnt vmcnt(0)" ::: "memory");
        LAS char* tp = lds + scr + (8 * (lane >> 4) + ((lane & 15) >> 2)) * 160 + 8 * (lane & 3);
        char* dst = ws + OFF_W2 + (size_t)((xcd * 68 + ks) * 5) * 1024 + lane * 16;
#pragma unroll
        for (int n = 0; n < 5; ++n) {
          s16x4 lo = tr_read(tp + n * 32), hi = tr_read(tp + n * 32 + 640);
          *(bf16x8*)(dst + n * 1024) = __builtin_shufflevector(lo, hi, 0, 1, 2, 3, 4, 5, 6, 7);
        }
      }
    }
    return;
  }

  const int tw = w & 1, ch = w >> 1;
  const int r = lane & 31, hh = lane >> 5;
  int xa[2][2];
  {
    int e = hh ^ ((r >> 1) & 7);
#pragma unroll
    for (int g = 0; g < 2; ++g)
#pragma unroll
      for (int ph = 0; ph < 2; ++ph) {
        int tg = 2 * tw + (g ^ ch), s = ph ^ tw;
        xa[g][ph] = K1_XOFF + (32 * tg + r) * 128 + (((2 * s) ^ e) << 4);
      }
  }
  int wa[2][3];
  {
    int G = lane >> 4, chh = G & 1, q = (lane & 15) >> 2, p = lane & 3;
#pragma unroll
    for (int ph = 0; ph < 2; ++ph) {
      int b0 = (8 * hh + q) * 128 + 32 * chh + 8 * p + (ph ^ tw) * 2048;
      wa[ph][0] = b0 + 64 * (0 ^ (q >> 1)) + ch * 2 * K1_WSLOT;
      wa[ph][1] = b0 + 64 * (1 ^ (q >> 1)) + ch * 2 * K1_WSLOT;
      wa[ph][2] = b0 + 64 * (ch ^ (q >> 1)) + 4 * K1_WSLOT;
    }
  }
  f32x16 acc[2][5];
#pragma unroll
  for (int g = 0; g < 2; ++g)
#pragma unroll
    for (int i = 0; i < 5; ++i)
#pragma unroll
      for (int j = 0; j < 16; ++j) acc[g][i][j] = 0.f;
  f32x4 cs[5];
#pragma unroll
  for (int i = 0; i < 5; ++i) cs[i] = f32x4{0.f, 0.f, 0.f, 0.f};
  bf16x8 onep;
  {
    const unsigned o = ((lane & 15) == ((lane >> 4) & 1)) ? 0x3f803f80u : 0u;
    u32x4 t = {o, o, o, o};
    onep = __builtin_bit_cast(bf16x8, t);
  }
  float s0 = 0.f, q0 = 0.f;

  bf16x8 xf[2];
  s16x4 wl[5], wh[5];
  auto rdx = [&](int i, int ph, int g) {
    LAS char* xb = lds + ((i / 2) % K1_NXS) * K1_XSLOT;
    xf[g] = *(LAS bf16x8*)(xb + (xa[g][ph] ^ ((i & 1) * 64)));
  };
  auto rdw = [&](int i, int ph, int c5) {
    LAS char* p = lds + (i % K1_NWS) * 4096 + ((c5 < 4) ? ((c5 >> 1) * K1_WSLOT + wa[ph][c5 & 1]) : wa[ph][2]);
    wl[c5] = tr_read(p);
    wh[c5] = tr_read(p + 512);
  };
  auto phase = [&](int i, int ph, int ni, int nph, bool do_rd) {
#pragma unroll
    for (int k = 0; k < 2 * NU; ++k) {
      const int g = (k >= NU) ? 1 : 0, c5 = k - g * NU;
      bf16x8 wfr = __builtin_shufflevector(wl[c5], wh[c5], 0, 1, 2, 3, 4, 5, 6, 7);
      if (TYPE == 2) acc[g][c5] = MFMA32(xf[g], wfr, acc[g][c5]);
      else acc[g][c5] = MFMA32(wfr, xf[g], acc[g][c5]);
      if (ph == 0 && g == 0) cs[c5] = MFMA16(wfr, onep, cs[c5]);
      if (k == 0) rdx(i, ph, 1);
      if (do_rd && g == 1) {
        if (c5 == 0) rdx(ni, nph, 0);
        rdw(ni, nph, c5);
      }
      if (k < 4) {
        u32x4 xu = __builtin_bit_cast(u32x4, xf[0]);
        float l = bf_lo(xu[k]), h = bf_hi(xu[k]);
        s0 += l; q0 = fmaf(l, l, q0); s0 += h; q0 = fmaf(h, h, q0);
        asm volatile("" : "+v"(s0), "+v"(q0));
      }
      __builtin_amdgcn_sched_barrier(0);
    }
  };

  unsigned pfd = 0, pfoff;
  const char* pfb;
  int pfstep;
  {
    const int rbi = rb & 7, nti = nt & 3;
    if (w == 3) {
      pfb = x + (size_t)(rb * 128 + 32 * nti) * 1280; pfoff = (lane & 31) * 1280; pfstep = 64;
    } else if (TYPE == 0) {
      if (w == 2) { pfb = wqkv + qcol0 * 2; pfoff = (4 * rbi + (lane & 3)) * 3840; pfstep = 32 * 3840; }
      else { pfb = (w ? wu : wg) + (2 * nt) * 128; pfoff = (4 * rbi + (lane & 3)) * 3072 + ((lane >> 2) & 1) * 128; pfstep = 32 * 3072; }
    } else {
      int u = w * 2 + ((lane >> 2) & 1); if (u >= NU) u = NU - 1;
      pfb = wqkv + qcol0 * 2; pfoff = (4 * rbi + (lane & 3)) * 3840 + u * 128; pfstep = 32 * 3840;
    }
  }
  auto pf = [&](int slot) {
  };
#pragma unroll
  for (int s_ = 4; s_ < PFD; ++s_) pf(s_);
  bar_only();
  rdx(0, 0, 0);
#pragma unroll
  for (int c5 = 0; c5 < NU; ++c5) rdw(0, 0, c5);
  __builtin_amdgcn_sched_barrier(0);
#pragma unroll 1
  for (int it = 0; it < 5; ++it) {
#pragma unroll
    for (int i = 0; i < 4; ++i) {
      phase(i, 0, i, 1, true);
      bar_only();
      __builtin_amdgcn_sched_barrier(0);
      phase(i, 1, (i + 1) & 3, 0, true);
    }
  }

  int tz = threadIdx.x;
  asm volatile("" : "+v"(tz));
  const int lane_ = tz & 63, r_ = tz & 31, hh_ = (tz >> 5) & 1;
#define lane lane_
#define r r_
#define hh hh_
  LAS float* csb = (LAS float*)(lds + K1_CS) + ch * 160;
  const __amdgpu_buffer_rsrc_t wsr = __builtin_amdgcn_make_buffer_rsrc(ws, 0, 0x7fffffff, 0x00020000);
  {
    const float ssum = swap_sum(s0), ssq = swap_sum(q0);
    const float mean = ssum * (1.f / 640.f);
    const float var = fmaxf(ssq * (1.f / 640.f) - mean * mean, 0.f);
    const float rs = __builtin_amdgcn_rsqf(var + 1e-5f);
    if (lane < 32) {
      f32x2 st = {-mean, rs};
      *(LAS f32x2*)(lds + K1_ST + (32 * (2 * tw + ch) + lane) * 8) = st;
    }
    if ((lane & 15) < 2) {
      LAS float* cp = csb + tw * 320 + 16 * (lane & 15) + 4 * (lane >> 4);
#pragma unroll
      for (int c5 = 0; c5 < NU; ++c5) *(LAS f32x4*)(cp + c5 * 32) = cs[c5];
    }
  }
  asm volatile("s_waitcnt lgkmcnt(0)\n\ts_barrier" ::: "memory");

  if (TYPE != 2) {
    f32x2 st[2];
#pragma unroll
    for (int g = 0; g < 2; ++g) st[g] = *(LAS f32x2*)(lds + K1_ST + (32 * (2 * tw + (g ^ ch)) + r) * 8);
#pragma unroll
    for (int c5 = 0; c5 < NU; ++c5) {
      f32x4 c4[4];
#pragma unroll
      for (int q4 = 0; q4 < 4; ++q4) c4[q4] = *(LAS f32x4*)(csb + c5 * 32 + 8 * q4 + 4 * hh) + *(LAS f32x4*)(csb + 320 + c5 * 32 + 8 * q4 + 4 * hh);
#pragma unroll
      for (int g = 0; g < 2; ++g) {
        const f32x2 nm2 = {st[g][0], st[g][0]};
#pragma unroll
        for (int q4 = 0; q4 < 4; ++q4)
#pragma unroll
          for (int j = 0; j < 2; ++j) {
            f32x2 v = {acc[g][c5][4 * q4 + 2 * j], acc[g][c5][4 * q4 + 2 * j + 1]};
            v = nm2 * f32x2{c4[q4][2 * j], c4[q4][2 * j + 1]} + v;
            acc[g][c5][4 * q4 + 2 * j] = v[0]; acc[g][c5][4 * q4 + 2 * j + 1] = v[1];
          }
      }
    }
#pragma unroll
    for (int g = 0; g < 2; ++g) {
      const int tok = rb * 128 + 32 * (2 * tw + (g ^ ch)) + r;
      const float rs = st[g][1];
      auto store_qk = [&](const f32x16& a, int nb) {
        const unsigned kq = (unsigned)OFF_KQ + tok * 16 + (nb / 8 + hh) * 32768;
        const float rsq = (nb < 640) ? rs * QSCALE : rs;
        const f32x2 rs2 = {rsq, rsq};
        unsigned P[8];
#pragma unroll
        for (int j = 0; j < 8; ++j) {
          f32x2 v = f32x2{a[2 * j], a[2 * j + 1]} * rs2;
          P[j] = pk2(v[0], v[1]);
        }
#pragma unroll
        for (int qp = 0; qp < 2; ++qp) {
          auto r0 = __builtin_amdgcn_permlane32_swap(P[4 * qp], P[4 * qp + 2], false, false);
          auto r1 = __builtin_amdgcn_permlane32_swap(P[4 * qp + 1], P[4 * qp + 3], false, false);
          u32x4 v = {r0[0], r1[0], r0[1], r1[1]};
          st_ws(wsr, kq + qp * 65536, v);
        }
      };
      if (TYPE == 0) {
        store_qk(acc[g][4], qcol0 + 32 * ch);
        const unsigned ht = (unsigned)OFF_HT + ((tok >> 4) * 48 * 64 + (tok & 15) + 16 * hh) * 16 + (2 * (2 * nt + ch)) * 1024;
        const float k1 = -1.4426950408889634f * rs;
        const f32x2 k12 = {k1, k1};
        const float irs2 = __builtin_amdgcn_rcpf(rs * rs);
        const f32x2 ir2 = {irs2, irs2};
#pragma unroll
        for (int c = 0; c < 2; ++c) {
          unsigned P[8];
#pragma unroll
          for (int j = 0; j < 8; ++j) {
            f32x2 gg = {acc[g][c][2 * j], acc[g][c][2 * j + 1]};
            f32x2 uu = {acc[g][2 + c][2 * j], acc[g][2 + c][2 * j + 1]};
            f32x2 t = gg * k12;
            f32x2 e = {__builtin_amdgcn_exp2f(t[0]), __builtin_amdgcn_exp2f(t[1])};
            f32x2 d = e * ir2 + ir2;
            f32x2 rr = {__builtin_amdgcn_rcpf(d[0]), __builtin_amdgcn_rcpf(d[1])};
            f32x2 hv = (gg * uu) * rr;
            P[j] = pk2(hv[0], hv[1]);
          }
#pragma unroll
          for (int qp = 0; qp < 2; ++qp) {
            auto r0 = __builtin_amdgcn_permlane32_swap(P[4 * qp], P[4 * qp + 2], false, false);
            auto r1 = __builtin_amdgcn_permlane32_swap(P[4 * qp + 1], P[4 * qp + 3], false, false);
            u32x4 v = {r0[0], r1[0], r0[1], r1[1]};
            st_ws(wsr, ht + c * 1024 + qp * 512, v);
          }
        }
      } else {
#pragma unroll
        for (int c5 = 0; c5 < 4; ++c5) store_qk(acc[g][c5], qcol0 + 128 * ch + 32 * c5);
      }
    }
  } else {
    int voff[5];
    float cn[5];
#pragma unroll
    for (int c5 = 0; c5 < 5; ++c5) {
      const int nv = qcol0 - 1280 + ((c5 < 4) ? (128 * ch + 32 * c5) : (256 + 32 * ch)) + r;
      const int head = nv / 20, d = nv - head * 20;
      voff[c5] = head * VT_HEAD + hh * 336 + d * 16;
      cn[c5] = csb[c5 * 32 + r] + csb[320 + c5 * 32 + r];
    }
#pragma unroll
    for (int g = 0; g < 2; ++g) {
      const int tb = 32 * (2 * tw + (g ^ ch));
      f32x2 mm[8], rr[8];
#pragma unroll
      for (int q4 = 0; q4 < 4; ++q4) {
        f32x4 a = *(LAS f32x4*)(lds + K1_ST + (tb + 8 * q4 + 4 * hh) * 8);
        f32x4 b = *(LAS f32x4*)(lds + K1_ST + (tb + 8 * q4 + 4 * hh) * 8 + 16);
        mm[2 * q4] = f32x2{a[0], a[2]}; rr[2 * q4] = f32x2{a[1], a[3]};
        mm[2 * q4 + 1] = f32x2{b[0], b[2]}; rr[2 * q4 + 1] = f32x2{b[1], b[3]};
      }
      const unsigned vb = (unsigned)OFF_VT + ((rb * 128 + tb) >> 4) * VT_TG;
#pragma unroll
      for (int c5 = 0; c5 < 5; ++c5) {
        const f32x2 c2 = {cn[c5], cn[c5]};
#pragma unroll
        for (int s = 0; s < 2; ++s) {
          u32x4 v;
#pragma unroll
          for (int j = 0; j < 4; ++j) {
            f32x2 t = {acc[g][c5][8 * s + 2 * j], acc[g][c5][8 * s + 2 * j + 1]};
            t = (mm[4 * s + j] * c2 + t) * rr[4 * s + j];
            v[j] = pk2(t[0], t[1]);
          }
          st_ws(wsr, vb + voff[c5] + s * VT_TG, v);
        }
      }
    }
  }
#undef lane
#undef r
#undef hh
}

__global__ __launch_bounds__(512, 2) void k1_kernel(const char* __restrict__ x, const char* __restrict__ wqkv,
                                                    const char* __restrict__ wo, const char* __restrict__ wg,
                                                    const char* __restrict__ wu, const char* __restrict__ wd,
                                                    char* __restrict__ ws) {
  __shared__ __attribute__((aligned(16))) char smem[K1_LDS];
  LAS char* lds = (LAS char*)smem;
  const int b = blockIdx.x, xcd = b & 7, idx = b >> 3;
  const int rb = 8 * (xcd & 1) + (idx & 7), nt = 4 * (xcd >> 1) + (idx >> 3);
  if (b == 0 && threadIdx.x < 8) *(unsigned*)(ws + OFF_CNT + threadIdx.x * 256) = 0u;
  if (nt < 12) k1_body<0>(x, wqkv, wg, wu, wd, wo, ws, lds, rb, nt);
  else if (nt < 14) k1_body<1>(x, wqkv, wg, wu, wd, wo, ws, lds, rb, nt);
  else k1_body<2>(x, wqkv, wg, wu, wd, wo, ws, lds, rb, nt);
}

#define PSLEEP 2
#define GSLEEP 8
constexpr int K2_K = 0;
constexpr int K2_Q = 24576;
constexpr int K2_V = 36864;
constexpr int K2_FLAG = K2_V + 21504;
constexpr int K2_RED = K2_FLAG + 64;
constexpr int RED_ROW = 336;
constexpr int K2_LDS = K2_RED + 4 * 64 * RED_ROW;

__global__ __launch_bounds__(768, 3) void k2_kernel(const char* __restrict__ x, char* __restrict__ ws,
                                                    char* __restrict__ out) {
  __shared__ __attribute__((aligned(16))) char smem[K2_LDS];
  LAS char* lds = (LAS char*)smem;
  const int tid = threadIdx.x, lane = tid & 63;
  const int w = __builtin_amdgcn_readfirstlane(tid >> 6);
  const int b = blockIdx.x, xcd = b & 7, idx = b >> 3;
  LAS unsigned* flg = (LAS unsigned*)(lds + K2_FLAG);
  if (tid < 8) flg[tid] = 0u;
  asm volatile("s_waitcnt lgkmcnt(0)\n\ts_barrier" ::: "memory");

  if (w < 8) {
    const int head = 4 * xcd + (idx & 3), rb = idx >> 2;
    const int r = lane & 31, hh = lane >> 5;
    {
      const unsigned l16 = lane * 16;
      const unsigned ldsb = (unsigned)(size_t)lds;
      auto dma = [&](const char* sbase, unsigned dst) {
        asm volatile("s_mov_b32 m0, %0\n\ts_nop 0\n\tglobal_load_lds_dwordx4 %1, %2" ::"s"(dst), "v"(l16), "s"(sbase) : "memory", "m0");
      };
      const char* qb_ = ws + OFF_KQ + (size_t)((head * 5) >> 1) * 32768 + rb * 4096 + (w & 3) * 1024;
      dma(qb_ + (w >> 2) * 32768, ldsb + K2_Q + w * 1024);
      dma(qb_ + 2 * 32768, ldsb + K2_Q + (8 + (w & 3)) * 1024);
      const char* kb_ = ws + OFF_KQ + (size_t)(80 + ((head * 5) >> 1)) * 32768 + rb * 4096 - 4096 + w * 1024;
      dma(kb_, ldsb + K2_K + w * 1024);
      dma(kb_ + 32768, ldsb + K2_K + (8 + w) * 1024);
      dma(kb_ + 65536, ldsb + K2_K + (16 + w) * 1024);
      const char* vb_ = ws + OFF_VT + (size_t)head * VT_HEAD + (16 * rb - 16) * VT_TG;
      const int j3 = (w + 16 <= 20) ? (w + 16) : w;
      dma(vb_ + w * 1024, ldsb + K2_V + w * 1024);
      dma(vb_ + (w + 8) * 1024, ldsb + K2_V + (w + 8) * 1024);
      dma(vb_ + j3 * 1024, ldsb + K2_V + j3 * 1024);
    }
    f32x16 mk0, mk8;
#pragma unroll
    for (int j = 0; j < 16; ++j) {
      const int kl = (j & 3) + 8 * (j >> 2) + 4 * hh;
      mk0[j] = (kl > r) ? 0.f : -INFINITY;
      mk8[j] = (kl <= r) ? 0.f : -INFINITY;
    }
    asm volatile("" : "+v"(mk0), "+v"(mk8));
    asm volatile("s_waitcnt vmcnt(3)" ::: "memory");
    if (lane == 0) __hip_atomic_fetch_add(flg, 1u, __ATOMIC_RELAXED, __HIP_MEMORY_SCOPE_WORKGROUP);
    for (int sp = 0; sp < (1 << 22); ++sp) {
      if (__hip_atomic_load(flg, __ATOMIC_RELAXED, __HIP_MEMORY_SCOPE_WORKGROUP) >= 8u) break;
      __builtin_amdgcn_s_sleep(1);
    }
    asm volatile("" ::: "memory");
    const int dcl = (r < 20) ? r : 20;
    const int qt = w;
    const int tmin = (rb == 0) ? (8 - qt) : 0;
    bf16x8 qa, qb;
    {
      LAS char* qp = lds + K2_Q + (32 * qt + r) * 16;
      qa = *(LAS bf16x8*)(qp + (hh + (head & 1)) * 4096);
      u32x2 c = *(LAS u32x2*)(qp + ((head & 1) ? 8 : 2 * 4096));
      u32x4 t1_ = {hh ? 0u : c[0], hh ? 0u : c[1], 0u, 0u};
      qb = __builtin_bit_cast(bf16x8, t1_);
    }
    LAS char* kp = lds + K2_K + (32 * qt + r) * 16;
    LAS char* kpa = kp + (hh + (head & 1)) * 8192;
    LAS char* kpc = kp + ((head & 1) ? 8 : 2 * 8192);
    bf16x8 ka[3], kc[3], vf[3][2];
    auto rdk = [&](int t) {
      ka[t % 3] = *(LAS bf16x8*)(kpa + t * 512);
      u32x2 c_ = *(LAS u32x2*)(kpc + t * 512);
      kc[t % 3] = __builtin_bit_cast(bf16x8, u32x4{c_[0], c_[1], 0u, 0u});
    };
    f32x16 sa, sb;
    float nm;
    {
      f32x16 z;
#pragma unroll
      for (int j = 0; j < 16; ++j) z[j] = 0.f;
      rdk(8); rdk(7);
      float mx[4];
      __builtin_amdgcn_sched_barrier(0);
#pragma unroll
      for (int t = 8; t >= -1; --t) {
        if (t >= 0) {
          f32x16 c0 = (t == 8) ? mk8 : ((t == 0) ? mk0 : z);
          f32x16 v = MFMA32(ka[t % 3], qa, c0);
          if (t - 2 >= 0) rdk(t - 2);
          v = MFMA32(kc[t % 3], qb, v);
          if (t & 1) sb = v; else sa = v;
        }
        if (t < 8) {
          const f32x16& p = ((t + 1) & 1) ? sb : sa;
          if (t + 1 == 8) {
#pragma unroll
            for (int u = 0; u < 4; ++u) mx[u] = fmaxf(fmaxf(p[4 * u], p[4 * u + 1]), fmaxf(p[4 * u + 2], p[4 * u + 3]));
          } else if (t + 1 >= tmin) {
#pragma unroll
            for (int j = 0; j < 16; j += 8)
#pragma unroll
              for (int u = 0; u < 4; ++u) mx[u] = fmaxf(fmaxf(mx[u], p[j + 2 * u]), p[j + 2 * u + 1]);
          }
          asm volatile("" : "+v"(mx[0]), "+v"(mx[1]), "+v"(mx[2]), "+v"(mx[3]));
        }
        __builtin_amdgcn_sched_barrier(0);
      }
      nm = -swap_max(fmaxf(fmaxf(mx[0], mx[1]), fmaxf(mx[2], mx[3])));
    }
    asm volatile("s_waitcnt vmcnt(0) lgkmcnt(0)" ::: "memory");
    {
      const int o1 = (lane >> 1) * VT_TG + (lane & 1) * 336 + 320;
      if (((o1 >> 10) & 7) == w) {
        u32x4 one = {0x3f803f80u, 0x3f803f80u, 0x3f803f80u, 0x3f803f80u};
        *(LAS u32x4*)(lds + K2_V + o1) = one;
      }
      asm volatile("s_waitcnt lgkmcnt(0)" ::: "memory");
    }
    if (lane == 0) __hip_atomic_fetch_add(flg + 2, 1u, __ATOMIC_RELAXED, __HIP_MEMORY_SCOPE_WORKGROUP);
    rdk(8); rdk(7);
    f32x16 cm;
#pragma unroll
    for (int j = 0; j < 16; ++j) { cm[j] = nm; mk0[j] += nm; mk8[j] += nm; }
    for (int sp = 0; sp < (1 << 22); ++sp) {
      if (__hip_atomic_load(flg + 2, __ATOMIC_RELAXED, __HIP_MEMORY_SCOPE_WORKGROUP) >= 8u) break;
      __builtin_amdgcn_s_sleep(1);
    }
    asm volatile("" ::: "memory");
    {
      LAS char* vp = lds + K2_V + (2 * qt) * VT_TG + dcl * 16 + hh * 336;
      auto rdv = [&](int t) {
        vf[t % 3][0] = *(LAS bf16x8*)(vp + (2 * t) * VT_TG);
        vf[t % 3][1] = *(LAS bf16x8*)(vp + (2 * t + 1) * VT_TG);
      };
      rdv(8);
      f32x16 o;
#pragma unroll
      for (int j = 0; j < 16; ++j) o[j] = 0.f;
      u32x4 pp[2];
      __builtin_amdgcn_sched_barrier(0);
#pragma unroll
      for (int t = 8; t >= -2; --t) {
        const bool ex = (t + 1 <= 8 && t + 1 >= 0);
        f32x16& p = ((t + 1) & 1) ? sb : sa;
        f32x16& v = (t & 1) ? sb : sa;
        auto exps = [&](int k) {
          if (ex) {
#pragma unroll
            for (int j = 4 * k; j < 4 * k + 4; ++j) p[j] = __builtin_amdgcn_exp2f(p[j]);
            asm volatile("" : "+v"(p));
          }
          __builtin_amdgcn_sched_barrier(0);
        };
        const bool pv = (t + 2 <= 8) && (t + 2 >= tmin);
        if (t + 2 <= 8) { if (pv) o = MFMA32(vf[(t + 2) % 3][0], __builtin_bit_cast(bf16x8, pp[0]), o); }
        exps(0);
        if (t + 2 <= 8) { if (pv) o = MFMA32(vf[(t + 2) % 3][1], __builtin_bit_cast(bf16x8, pp[1]), o); }
        if (t - 1 >= 0) rdv(t - 1);
        exps(1);
        if (t >= 0) {
          f32x16 c0 = (t == 8) ? mk8 : ((t == 0) ? mk0 : cm);
          v = MFMA32(ka[t % 3], qa, c0);
        }
        exps(2);
        if (t >= 0) {
          v = MFMA32(kc[t % 3], qb, v);
          if (t - 2 >= 0) rdk(t - 2);
        }
        exps(3);
        if (ex) {
#pragma unroll
          for (int s = 0; s < 2; ++s)
#pragma unroll
            for (int j = 0; j < 4; ++j) pp[s][j] = pk2(p[8 * s + 2 * j], p[8 * s + 2 * j + 1]);
          asm volatile("" : "+v"(pp[0]), "+v"(pp[1]));
        }
        __builtin_amdgcn_sched_barrier(0);
      }
      const float rl = __builtin_amdgcn_rcpf(o[12]);
      const int tok = 256 * rb + 32 * qt + r;
      char* ob = ws + OFF_OT + ((size_t)(tok >> 4) * 20 * 64 + (tok & 15)) * 16;
#pragma unroll
      for (int a = 0; a < 3; ++a) {
        const int k = head * 20 + 8 * a + 4 * hh;
        unsigned long long v = (unsigned long long)pk2(o[4 * a] * rl, o[4 * a + 1] * rl) |
                               ((unsigned long long)pk2(o[4 * a + 2] * rl, o[4 * a + 3] * rl) << 32);
        char* p = ob + (size_t)(k >> 5) * 1024 + ((k >> 3) & 3) * 256 + (k & 7) * 2;
        if (a < 2 || hh == 0)
          __hip_atomic_store((unsigned long long*)p, v, __ATOMIC_RELAXED, __HIP_MEMORY_SCOPE_AGENT);
      }
    }
    asm volatile("s_waitcnt vmcnt(0)" ::: "memory");
    if (lane == 0) {
      unsigned old = __hip_atomic_fetch_add(flg + 1, 1u, __ATOMIC_RELAXED, __HIP_MEMORY_SCOPE_WORKGROUP);
      if (old == 7u)
        __hip_atomic_fetch_add((unsigned*)(ws + OFF_CNT + rb * 256), 1u, __ATOMIC_RELAXED, __HIP_MEMORY_SCOPE_AGENT);
    }
    return;
  }

  const int kw = w - 8;
  const int rb64 = 8 * (xcd >> 1) + (idx & 7), ct = 4 * (xcd & 1) + (idx >> 3);
  const char* wp = ws + OFF_W2 + (size_t)ct * 68 * 5 * 1024 + lane * 16;
  const char* hp = ws + OFF_HT + (size_t)(4 * rb64) * 48 * 1024 + lane * 16;
  auto orsrc = __builtin_amdgcn_make_buffer_rsrc((void*)(ws + OFF_OT), 0, (int)SZ_OT, 0x00020000);
  const int ovoff = (4 * rb64) * 20 * 1024 + lane * 16;
  f32x4 acc[5][4];
#pragma unroll
  for (int n = 0; n < 5; ++n)
#pragma unroll
    for (int i = 0; i < 4; ++i) acc[n][i] = f32x4{0.f, 0.f, 0.f, 0.f};
  constexpr int GD = 2;
  bf16x8 bw[GD][5], bt[GD][4];
  auto loadT = [&](int st, int sl) {
    if (st < 12) {
      const int ks = 12 * kw + st;
#pragma unroll
      for (int i = 0; i < 4; ++i) bt[sl][i] = *(const bf16x8*)(hp + (size_t)(i * 48 + ks) * 1024);
    } else {
      const int ko = 5 * kw + (st - 12);
#pragma unroll
      for (int i = 0; i < 4; ++i)
        bt[sl][i] = __builtin_bit_cast(bf16x8, __builtin_amdgcn_raw_buffer_load_b128(orsrc, ovoff + (i * 20 + ko) * 1024, 0, 16));
    }
    __builtin_amdgcn_sched_barrier(0);
  };
  auto loadW = [&](int st, int sl) {
    const int ks = (st < 12) ? (12 * kw + st) : (48 + 5 * kw + (st - 12));
#pragma unroll
    for (int n = 0; n < 5; ++n) bw[sl][n] = *(const bf16x8*)(wp + (size_t)(ks * 5 + n) * 1024);
    __builtin_amdgcn_sched_barrier(0);
  };
  auto mm = [&](int st) {
#pragma unroll
    for (int n = 0; n < 5; ++n)
#pragma unroll
      for (int i = 0; i < 4; ++i) acc[n][i] = MFMA16(bw[st % GD][n], bt[st % GD][i], acc[n][i]);
    __builtin_amdgcn_sched_barrier(0);
  };
  __builtin_amdgcn_s_sleep(GSLEEP);
#pragma unroll
  for (int st = 0; st < GD; ++st) { loadT(st, st); loadW(st, st); }
#pragma unroll
  for (int st = 0; st < 12; ++st) {
    mm(st);
    if (st + GD < 12) loadT(st + GD, st % GD);
    loadW(st + GD, st % GD);
  }
  {
    if (kw == 0) {
      const unsigned* cnt = (const unsigned*)(ws + OFF_CNT + (rb64 >> 2) * 256);
      for (int sp = 0; sp < (1 << 22); ++sp) {
        if (__hip_atomic_load(cnt, __ATOMIC_RELAXED, __HIP_MEMORY_SCOPE_AGENT) >= 32u) break;
        __builtin_amdgcn_s_sleep(PSLEEP);
      }
      __hip_atomic_store(flg + 3, 1u, __ATOMIC_RELAXED, __HIP_MEMORY_SCOPE_WORKGROUP);
    } else {
      for (int sp = 0; sp < (1 << 24); ++sp) {
        if (__hip_atomic_load(flg + 3, __ATOMIC_RELAXED, __HIP_MEMORY_SCOPE_WORKGROUP) != 0u) break;
        __builtin_amdgcn_s_sleep(1);
      }
    }
    __builtin_amdgcn_fence(__ATOMIC_ACQUIRE, "wavefront");
    asm volatile("" ::: "memory");
  }
  loadT(12, 0); loadT(13, 1);
  mm(12); loadT(14, 0); loadW(14, 0);
  mm(13); loadT(15, 1); loadW(15, 1);
  mm(14); loadT(16, 0); loadW(16, 0);
  mm(15);
  mm(16);
  const int tq = tid - 512;
  u32x4 xv[3];
#pragma unroll
  for (int it = 0; it < 3; ++it) {
    const int item = tq + 256 * it;
    const int tk = item / 10, n8 = item - tk * 10;
    if (item < 640) xv[it] = *(const u32x4*)(x + (size_t)(64 * rb64 + tk) * 1280 + (80 * ct + 8 * n8) * 2);
  }
  {
    LAS char* rp = lds + K2_RED + (kw * 64 + (lane & 15)) * RED_ROW + (lane >> 4) * 16;
#pragma unroll
    for (int n = 0; n < 5; ++n)
#pragma unroll
      for (int i = 0; i < 4; ++i) *(LAS f32x4*)(rp + i * 16 * RED_ROW + n * 64) = acc[n][i];
  }
  asm volatile("s_waitcnt lgkmcnt(0)\n\ts_barrier" ::: "memory");
#pragma unroll
  for (int it = 0; it < 3; ++it) {
    const int item = tq + 256 * it;
    const int tk = item / 10, n8 = item - tk * 10;
    if (item < 640) {
      LAS char* rp = lds + K2_RED + tk * RED_ROW + n8 * 32;
      f32x4 a = *(LAS f32x4*)rp, c = *(LAS f32x4*)(rp + 16);
#pragma unroll
      for (int k = 1; k < 4; ++k) {
        a += *(LAS f32x4*)(rp + k * 64 * RED_ROW);
        c += *(LAS f32x4*)(rp + k * 64 * RED_ROW + 16);
      }
      u32x4 xx = xv[it];
      u32x4 v;
      v[0] = pk2(a[0] + __uint_as_float(xx[0] << 16), a[1] + __uint_as_float(xx[0] & 0xffff0000u));
      v[1] = pk2(a[2] + __uint_as_float(xx[1] << 16), a[3] + __uint_as_float(xx[1] & 0xffff0000u));
      v[2] = pk2(c[0] + __uint_as_float(xx[2] << 16), c[1] + __uint_as_float(xx[2] & 0xffff0000u));
      v[3] = pk2(c[2] + __uint_as_float(xx[3] << 16), c[3] + __uint_as_float(xx[3] & 0xffff0000u));
      *(u32x4*)(out + (size_t)(64 * rb64 + tk) * 1280 + (80 * ct + 8 * n8) * 2) = v;
    }
  }
}

}

extern "C" void kernel_launch(void* const* d_in, const int* in_sizes, int n_in, void* d_out, int out_size,
                              void* d_ws, size_t ws_size, hipStream_t stream) {
  const char* x = (const char*)d_in[0];
  const char* wqkv = (const char*)d_in[1];
  const char* wo = (const char*)d_in[2];
  const char* wg = (const char*)d_in[4];
  const char* wu = (const char*)d_in[5];
  const char* wd = (const char*)d_in[6];
  k1_kernel<<<256, 512, 0, stream>>>(x, wqkv, wo, wg, wu, wd, (char*)d_ws);
  k2_kernel<<<256, 768, 0, stream>>>(x, (char*)d_ws, (char*)d_out);
}
```

```cpp
#include <hip/hip_runtime.h>
#include <hip/hip_bf16.h>
#include <stdint.h>

typedef short bf16x8 __attribute__((ext_vector_type(8)));
typedef short s16x4 __attribute__((ext_vector_type(4)));
typedef float f32x16 __attribute__((ext_vector_type(16)));
typedef float f32x4 __attribute__((ext_vector_type(4)));
typedef float f32x2 __attribute__((ext_vector_type(2)));
typedef unsigned u32x4 __attribute__((ext_vector_type(4)));
typedef unsigned u32x2 __attribute__((ext_vector_type(2)));
typedef __bf16 bf2 __attribute__((ext_vector_type(2)));
#define LAS __attribute__((address_space(3)))
#define DI __device__ __forceinline__

namespace {

constexpr float QSCALE = 0.22360679774997896f * 1.4426950408889634f;

constexpr size_t OFF_CNT = 0;
constexpr size_t OFF_KQ = 32768;
constexpr size_t SZ_KQ = 160ull * 2048 * 16;
constexpr size_t OFF_VT = OFF_KQ + SZ_KQ;
constexpr int VT_TG = 21 * 32;
constexpr int VT_HEAD = 128 * VT_TG;
constexpr size_t SZ_VT = 32ull * VT_HEAD;
constexpr size_t OFF_HT = OFF_VT + SZ_VT;
constexpr size_t SZ_HT = 128ull * 48 * 1024;
constexpr size_t OFF_OT = OFF_HT + SZ_HT;
constexpr size_t SZ_OT = 128ull * 20 * 1024;
constexpr size_t OFF_W2 = OFF_OT + SZ_OT;

DI float swap_sum(float x) {
  auto r = __builtin_amdgcn_permlane32_swap(__float_as_uint(x), __float_as_uint(x), false, false);
  return __uint_as_float(r[0]) + __uint_as_float(r[1]);
}
DI float swap_max(float x) {
  auto r = __builtin_amdgcn_permlane32_swap(__float_as_uint(x), __float_as_uint(x), false, false);
  return fmaxf(__uint_as_float(r[0]), __uint_as_float(r[1]));
}
DI unsigned pk2(float a, float b) {
  f32x2 v = {a, b};
  return __builtin_bit_cast(unsigned, __builtin_convertvector(v, bf2));
}
DI s16x4 tr_read(LAS char* p) {
  return __builtin_amdgcn_ds_read_tr16_b64_v4i16((LAS s16x4*)p);
}
template <int N> DI void wait_bar() {
  asm volatile("s_waitcnt vmcnt(%0) lgkmcnt(0)\n\ts_barrier" ::"i"(N) : "memory");
}
#define MFMA32(a, b, c) __builtin_amdgcn_mfma_f32_32x32x16_bf16((a), (b), (c), 0, 0, 0)
#define MFMA16(a, b, c) __builtin_amdgcn_mfma_f32_16x16x32_bf16((a), (b), (c), 0, 0, 0)

constexpr int K1_WSLOT = 20480, K1_NWS = 5;
constexpr int K1_XOFF = K1_WSLOT * K1_NWS;
constexpr int K1_XSLOT = 16384, K1_NXS = 3;
constexpr int K1_CS = K1_XOFF + K1_XSLOT * K1_NXS;
constexpr int K1_ST = K1_CS + 2560;
constexpr int K1_LDS = K1_ST + 1024;

DI float bf_lo(unsigned v) { return __uint_as_float(v << 16); }
DI float bf_hi(unsigned v) { return __uint_as_float(v & 0xffff0000u); }
DI void bar_only() { asm volatile("s_barrier" ::: "memory"); }

DI void st_ws(__amdgpu_buffer_rsrc_t rs, unsigned off, u32x4 v) { __builtin_amdgcn_raw_buffer_store_b128(v, rs, (int)off, 0, 0); }
DI void st_ws(__amdgpu_buffer_rsrc_t rs, unsigned off, u32x2 v) { __builtin_amdgcn_raw_buffer_store_b64(v, rs, (int)off, 0, 0); }

template <int TYPE>
DI void k1_body(const char* __restrict__ x, const char* __restrict__ wqkv, const char* __restrict__ wg,
                const char* __restrict__ wu, const char* __restrict__ wd, const char* __restrict__ wo,
                char* __restrict__ ws, LAS char* lds, int rb, int nt) {
  constexpr int NU = (TYPE == 1) ? 4 : 5;
  const int tid = threadIdx.x, lane = tid & 63;
  const int w = __builtin_amdgcn_readfirstlane(tid >> 6);
  const int qcol0 = (TYPE == 0) ? nt * 64 : (TYPE == 1) ? (12 + 4 * (nt - 12)) * 64 : 1280 + 5 * (nt - 14) * 64;

  if (w >= 4) {
    const int hw = w - 4;
    const char* ub[5];
    if (TYPE == 0) {
      ub[0] = wg + (2 * nt) * 128; ub[1] = wu + (2 * nt) * 128;
      ub[2] = wg + (2 * nt + 1) * 128; ub[3] = wu + (2 * nt + 1) * 128;
      ub[4] = wqkv + qcol0 * 2;
    } else {
      for (int u = 0; u < 5; ++u) ub[u] = wqkv + (qcol0 + 64 * u) * 2;
    }
    unsigned voffA, voffB, xvoff0, xvoff1;
    {
      int row = 8 * hw + (lane >> 3);
      int cc = ((((lane >> 2) & 1) ^ ((row >> 1) & 1)) << 2) + (lane & 3);
      voffA = row * 3072 + cc * 16;
      voffB = row * 3840 + cc * 16;
      int xr = rb * 128 + 32 * hw + (lane >> 3);
      int sc = (lane & 7) ^ (lane >> 4);
      xvoff0 = xr * 1280 + sc * 16;
      xvoff1 = xr * 1280 + (sc ^ 4) * 16;
    }
    const unsigned ldsb = (unsigned)(size_t)lds;
    auto dma = [&](const char* sbase, unsigned voff, unsigned dst) {
      asm volatile("s_mov_b32 m0, %0\n\ts_nop 0\n\tglobal_load_lds_dwordx4 %1, %2" ::"s"(dst), "v"(voff), "s"(sbase) : "memory", "m0");
    };
    const int coff = (blockIdx.x & 1) * 5 + ((blockIdx.x >> 1) & 3);
    auto issueW = [&](int slot) {
      const unsigned dst = ldsb + (slot % K1_NWS) * 4096 + hw * 1024;
      int gs = slot + 2 * coff; if (gs >= 20) gs -= 20;
#pragma unroll
      for (int u = 0; u < NU; ++u) {
        const bool a = (TYPE == 0 && u < 4);
        dma(ub[u] + (size_t)gs * 32 * (a ? 3072 : 3840), a ? voffA : voffB, dst + u * K1_WSLOT);
      }
    };
    auto issueX = [&](int c) {
      const unsigned dst = ldsb + K1_XOFF + (c % K1_NXS) * K1_XSLOT + hw * 4096;
      int gc = c + coff; if (gc >= 10) gc -= 10;
#pragma unroll
      for (int i = 0; i < 4; ++i) dma(x + gc * 128 + i * 8 * 1280, (i & 1) ? xvoff1 : xvoff0, dst + i * 1024);
    };
    issueW(0); issueX(0); issueW(1); issueW(2); issueX(1);
    wait_bar<2 * NU + 4>();
    issueW(3);
#pragma unroll
    for (int i = 0; i < 19; ++i) {
      const int nw = ((i + 3 < 19) ? (i + 3) : 19) - (i + 1);
      const bool hx = (i & 1) ? (i <= 15) : (i <= 16);
      const int cnt = NU * nw + (hx ? 4 : 0);
      if (cnt == 2 * NU + 4) wait_bar<2 * NU + 4>();
      else if (cnt == 2 * NU) wait_bar<2 * NU>();
      else if (cnt == NU) wait_bar<NU>();
      else wait_bar<0>();
      if (i + 4 < 20) issueW(i + 4);
      if ((i & 1) == 0 && (i / 2 + 2) < 10) issueX(i / 2 + 2);
    }
    bar_only();
    {
      const int xcd = blockIdx.x & 7, ks = (blockIdx.x >> 3) + 32 * hw;
      if (hw < 3 && ks < 68) {
        const char* src = (ks < 48) ? (wd + (size_t)(32 * ks) * 1280) : (wo + (size_t)(32 * (ks - 48)) * 1280);
        src += xcd * 160;
        const unsigned scr = hw * 5120;
#pragma unroll
        for (int j = 0; j < 5; ++j) {
          const int sl = j * 64 + lane, row = sl / 10, cc = sl - row * 10;
          dma(src, row * 1280 + cc * 16, ldsb + scr + j * 1024);
        }
        asm volatile("s_waitcnt vmcnt(0)" ::: "memory");
        LAS char* tp = lds + scr + (8 * (lane >> 4) + ((lane & 15) >> 2)) * 160 + 8 * (lane & 3);
        char* dst = ws + OFF_W2 + (size_t)((xcd * 68 + ks) * 5) * 1024 + lane * 16;
#pragma unroll
        for (int n = 0; n < 5; ++n) {
          s16x4 lo = tr_read(tp + n * 32), hi = tr_read(tp + n * 32 + 640);
          *(bf16x8*)(dst + n * 1024) = __builtin_shufflevector(lo, hi, 0, 1, 2, 3, 4, 5, 6, 7);
        }
      }
    }
    return;
  }

  const int tw = w & 1, ch = w >> 1;
  const int r = lane & 31, hh = lane >> 5;
  int xa[2][2];
  {
    int e = hh ^ ((r >> 1) & 7);
#pragma unroll
    for (int g = 0; g < 2; ++g)
#pragma unroll
      for (int ph = 0; ph < 2; ++ph) {
        int tg = 2 * tw + (g ^ ch), s = ph ^ tw;
        xa[g][ph] = K1_XOFF + (32 * tg + r) * 128 + (((2 * s) ^ e) << 4);
      }
  }
  int wa[2][3];
  {
    int G = lane >> 4, chh = G & 1, q = (lane & 15) >> 2, p = lane & 3;
#pragma unroll
    for (int ph = 0; ph < 2; ++ph) {
      int b0 = (8 * hh + q) * 128 + 32 * chh + 8 * p + (ph ^ tw) * 2048;
      wa[ph][0] = b0 + 64 * (0 ^ (q >> 1)) + ch * 2 * K1_WSLOT;
      wa[ph][1] = b0 + 64 * (1 ^ (q >> 1)) + ch * 2 * K1_WSLOT;
      wa[ph][2] = b0 + 64 * (ch ^ (q >> 1)) + 4 * K1_WSLOT;
    }
  }
  f32x16 acc[2][5];
#pragma unroll
  for (int g = 0; g < 2; ++g)
#pragma unroll
    for (int i = 0; i < 5; ++i)
#pragma unroll
      for (int j = 0; j < 16; ++j) acc[g][i][j] = 0.f;
  f32x4 cs[5];
#pragma unroll
  for (int i = 0; i < 5; ++i) cs[i] = f32x4{0.f, 0.f, 0.f, 0.f};
  bf16x8 onep;
  {
    const unsigned o = ((lane & 15) == ((lane >> 4) & 1)) ? 0x3f803f80u : 0u;
    u32x4 t = {o, o, o, o};
    onep = __builtin_bit_cast(bf16x8, t);
  }
  float s0 = 0.f, q0 = 0.f;

  bf16x8 xf[2];
  s16x4 wl[5], wh[5];
  auto rdx = [&](int i, int ph, int g) {
    LAS char* xb = lds + ((i / 2) % K1_NXS) * K1_XSLOT;
    xf[g] = *(LAS bf16x8*)(xb + (xa[g][ph] ^ ((i & 1) * 64)));
  };
  auto rdw = [&](int i, int ph, int c5) {
    LAS char* p = lds + (i % K1_NWS) * 4096 + ((c5 < 4) ? ((c5 >> 1) * K1_WSLOT + wa[ph][c5 & 1]) : wa[ph][2]);
    wl[c5] = tr_read(p);
    wh[c5] = tr_read(p + 512);
  };
  auto phase = [&](int i, int ph, int ni, int nph, bool do_rd) {
#pragma unroll
    for (int k = 0; k < 2 * NU; ++k) {
      const int g = (k >= NU) ? 1 : 0, c5 = k - g * NU;
      bf16x8 wfr = __builtin_shufflevector(wl[c5], wh[c5], 0, 1, 2, 3, 4, 5, 6, 7);
      if (TYPE == 2) acc[g][c5] = MFMA32(xf[g], wfr, acc[g][c5]);
      else acc[g][c5] = MFMA32(wfr, xf[g], acc[g][c5]);
      if (ph == 0 && g == 0) cs[c5] = MFMA16(wfr, onep, cs[c5]);
      if (k == 0) rdx(i, ph, 1);
      if (do_rd && g == 1) {
        if (c5 == 0) rdx(ni, nph, 0);
        rdw(ni, nph, c5);
      }
      if (k < 4) {
        u32x4 xu = __builtin_bit_cast(u32x4, xf[0]);
        float l = bf_lo(xu[k]), h = bf_hi(xu[k]);
        s0 += l; q0 = fmaf(l, l, q0); s0 += h; q0 = fmaf(h, h, q0);
        asm volatile("" : "+v"(s0), "+v"(q0));
      }
      __builtin_amdgcn_sched_barrier(0);
    }
  };

  bar_only();
  rdx(0, 0, 0);
#pragma unroll
  for (int c5 = 0; c5 < NU; ++c5) rdw(0, 0, c5);
  __builtin_amdgcn_sched_barrier(0);
#pragma unroll
  for (int i = 0; i < 20; ++i) {
    phase(i, 0, i, 1, true);
    if (i < 19) {
      bar_only();
      __builtin_amdgcn_sched_barrier(0);
      phase(i, 1, i + 1, 0, true);
    } else {
      phase(i, 1, 0, 0, false);
    }
  }

  int tz = threadIdx.x;
  asm volatile("" : "+v"(tz));
  const int lane_ = tz & 63, r_ = tz & 31, hh_ = (tz >> 5) & 1;
#define lane lane_
#define r r_
#define hh hh_
  LAS float* csb = (LAS float*)(lds + K1_CS) + ch * 160;
  const __amdgpu_buffer_rsrc_t wsr = __builtin_amdgcn_make_buffer_rsrc(ws, 0, 0x7fffffff, 0x00020000);
  {
    const float ssum = swap_sum(s0), ssq = swap_sum(q0);
    const float mean = ssum * (1.f / 640.f);
    const float var = fmaxf(ssq * (1.f / 640.f) - mean * mean, 0.f);
    const float rs = __builtin_amdgcn_rsqf(var + 1e-5f);
    if (lane < 32) {
      f32x2 st = {-mean, rs};
      *(LAS f32x2*)(lds + K1_ST + (32 * (2 * tw + ch) + lane) * 8) = st;
    }
    if ((lane & 15) < 2) {
      LAS float* cp = csb + tw * 320 + 16 * (lane & 15) + 4 * (lane >> 4);
#pragma unroll
      for (int c5 = 0; c5 < NU; ++c5) *(LAS f32x4*)(cp + c5 * 32) = cs[c5];
    }
  }
  asm volatile("s_waitcnt lgkmcnt(0)\n\ts_barrier" ::: "memory");

  if (TYPE != 2) {
    f32x2 st[2];
#pragma unroll
    for (int g = 0; g < 2; ++g) st[g] = *(LAS f32x2*)(lds + K1_ST + (32 * (2 * tw + (g ^ ch)) + r) * 8);
#pragma unroll
    for (int c5 = 0; c5 < NU; ++c5) {
      f32x4 c4[4];
#pragma unroll
      for (int q4 = 0; q4 < 4; ++q4) c4[q4] = *(LAS f32x4*)(csb + c5 * 32 + 8 * q4 + 4 * hh) + *(LAS f32x4*)(csb + 320 + c5 * 32 + 8 * q4 + 4 * hh);
#pragma unroll
      for (int g = 0; g < 2; ++g) {
        const f32x2 nm2 = {st[g][0], st[g][0]};
#pragma unroll
        for (int q4 = 0; q4 < 4; ++q4)
#pragma unroll
          for (int j = 0; j < 2; ++j) {
            f32x2 v = {acc[g][c5][4 * q4 + 2 * j], acc[g][c5][4 * q4 + 2 * j + 1]};
            v = nm2 * f32x2{c4[q4][2 * j], c4[q4][2 * j + 1]} + v;
            acc[g][c5][4 * q4 + 2 * j] = v[0]; acc[g][c5][4 * q4 + 2 * j + 1] = v[1];
          }
      }
    }
#pragma unroll
    for (int g = 0; g < 2; ++g) {
      const int tok = rb * 128 + 32 * (2 * tw + (g ^ ch)) + r;
      const float rs = st[g][1];
      auto store_qk = [&](const f32x16& a, int nb) {
        const unsigned kq = (unsigned)OFF_KQ + tok * 16 + (nb / 8 + hh) * 32768;
        const float rsq = (nb < 640) ? rs * QSCALE : rs;
        const f32x2 rs2 = {rsq, rsq};
        unsigned P[8];
#pragma unroll
        for (int j = 0; j < 8; ++j) {
          f32x2 v = f32x2{a[2 * j], a[2 * j + 1]} * rs2;
          P[j] = pk2(v[0], v[1]);
        }
#pragma unroll
        for (int qp = 0; qp < 2; ++qp) {
          auto r0 = __builtin_amdgcn_permlane32_swap(P[4 * qp], P[4 * qp + 2], false, false);
          auto r1 = __builtin_amdgcn_permlane32_swap(P[4 * qp + 1], P[4 * qp + 3], false, false);
          u32x4 v = {r0[0], r1[0], r0[1], r1[1]};
          st_ws(wsr, kq + qp * 65536, v);
        }
      };
      if (TYPE == 0) {
        store_qk(acc[g][4], qcol0 + 32 * ch);
        const unsigned ht = (unsigned)OFF_HT + ((tok >> 4) * 48 * 64 + (tok & 15) + 16 * hh) * 16 + (2 * (2 * nt + ch)) * 1024;
        const float k1 = -1.4426950408889634f * rs;
        const f32x2 k12 = {k1, k1};
        const float irs2 = __builtin_amdgcn_rcpf(rs * rs);
        const f32x2 ir2 = {irs2, irs2};
#pragma unroll
        for (int c = 0; c < 2; ++c) {
          unsigned P[8];
#pragma unroll
          for (int j = 0; j < 8; ++j) {
            f32x2 gg = {acc[g][c][2 * j], acc[g][c][2 * j + 1]};
            f32x2 uu = {acc[g][2 + c][2 * j], acc[g][2 + c][2 * j + 1]};
            f32x2 t = gg * k12;
            f32x2 e = {__builtin_amdgcn_exp2f(t[0]), __builtin_amdgcn_exp2f(t[1])};
            f32x2 d = e * ir2 + ir2;
            f32x2 rr = {__builtin_amdgcn_rcpf(d[0]), __builtin_amdgcn_rcpf(d[1])};
            f32x2 hv = (gg * uu) * rr;
            P[j] = pk2(hv[0], hv[1]);
          }
#pragma unroll
          for (int qp = 0; qp < 2; ++qp) {
            auto r0 = __builtin_amdgcn_permlane32_swap(P[4 * qp], P[4 * qp + 2], false, false);
            auto r1 = __builtin_amdgcn_permlane32_swap(P[4 * qp + 1], P[4 * qp + 3], false, false);
            u32x4 v = {r0[0], r1[0], r0[1], r1[1]};
            st_ws(wsr, ht + c * 1024 + qp * 512, v);
          }
        }
      } else {
#pragma unroll
        for (int c5 = 0; c5 < 4; ++c5) store_qk(acc[g][c5], qcol0 + 128 * ch + 32 * c5);
      }
    }
  } else {
    int voff[5];
    float cn[5];
#pragma unroll
    for (int c5 = 0; c5 < 5; ++c5) {
      const int nv = qcol0 - 1280 + ((c5 < 4) ? (128 * ch + 32 * c5) : (256 + 32 * ch)) + r;
      const int head = nv / 20, d = nv - head * 20;
      voff[c5] = head * VT_HEAD + hh * 336 + d * 16;
      cn[c5] = csb[c5 * 32 + r] + csb[320 + c5 * 32 + r];
    }
#pragma unroll
    for (int g = 0; g < 2; ++g) {
      const int tb = 32 * (2 * tw + (g ^ ch));
      f32x2 mm[8], rr[8];
#pragma unroll
      for (int q4 = 0; q4 < 4; ++q4) {
        f32x4 a = *(LAS f32x4*)(lds + K1_ST + (tb + 8 * q4 + 4 * hh) * 8);
        f32x4 b = *(LAS f32x4*)(lds + K1_ST + (tb + 8 * q4 + 4 * hh) * 8 + 16);
        mm[2 * q4] = f32x2{a[0], a[2]}; rr[2 * q4] = f32x2{a[1], a[3]};
        mm[2 * q4 + 1] = f32x2{b[0], b[2]}; rr[2 * q4 + 1] = f32x2{b[1], b[3]};
      }
      const unsigned vb = (unsigned)OFF_VT + ((rb * 128 + tb) >> 4) * VT_TG;
#pragma unroll
      for (int c5 = 0; c5 < 5; ++c5) {
        const f32x2 c2 = {cn[c5], cn[c5]};
#pragma unroll
        for (int s = 0; s < 2; ++s) {
          u32x4 v;
#pragma unroll
          for (int j = 0; j < 4; ++j) {
            f32x2 t = {acc[g][c5][8 * s + 2 * j], acc[g][c5][8 * s + 2 * j + 1]};
            t = (mm[4 * s + j] * c2 + t) * rr[4 * s + j];
            v[j] = pk2(t[0], t[1]);
          }
          st_ws(wsr, vb + voff[c5] + s * VT_TG, v);
        }
      }
    }
  }
#undef lane
#undef r
#undef hh
}

__global__ __launch_bounds__(512, 2) void k1_kernel(const char* __restrict__ x, const char* __restrict__ wqkv,
                                                    const char* __restrict__ wo, const char* __restrict__ wg,
                                                    const char* __restrict__ wu, const char* __restrict__ wd,
                                                    char* __restrict__ ws) {
  __shared__ __attribute__((aligned(16))) char smem[K1_LDS];
  LAS char* lds = (LAS char*)smem;
  const int b = blockIdx.x, xcd = b & 7, idx = b >> 3;
  const int rb = 8 * (xcd & 1) + (idx & 7), nt = 4 * (xcd >> 1) + (idx >> 3);
  if (b == 0 && threadIdx.x < 8) *(unsigned*)(ws + OFF_CNT + threadIdx.x * 256) = 0u;
  if (nt < 12) k1_body<0>(x, wqkv, wg, wu, wd, wo, ws, lds, rb, nt);
  else if (nt < 14) k1_body<1>(x, wqkv, wg, wu, wd, wo, ws, lds, rb, nt);
  else k1_body<2>(x, wqkv, wg, wu, wd, wo, ws, lds, rb, nt);
}

#define PSLEEP 2
#define GSLEEP 8
constexpr int K2_K = 0;
constexpr int K2_Q = 24576;
constexpr int K2_V = 36864;
constexpr int K2_FLAG = K2_V + 21504;
constexpr int K2_RED = K2_FLAG + 64;
constexpr int RED_ROW = 336;
constexpr int K2_LDS = K2_RED + 4 * 64 * RED_ROW;

__global__ __launch_bounds__(768, 3) void k2_kernel(const char* __restrict__ x, char* __restrict__ ws,
                                                    char* __restrict__ out) {
  __shared__ __attribute__((aligned(16))) char smem[K2_LDS];
  LAS char* lds = (LAS char*)smem;
  const int tid = threadIdx.x, lane = tid & 63;
  const int w = __builtin_amdgcn_readfirstlane(tid >> 6);
  const int b = blockIdx.x, xcd = b & 7, idx = b >> 3;
  LAS unsigned* flg = (LAS unsigned*)(lds + K2_FLAG);
  if (tid < 8) flg[tid] = 0u;
  asm volatile("s_waitcnt lgkmcnt(0)\n\ts_barrier" ::: "memory");

  if (w < 8) {
    const int head = 4 * xcd + (idx & 3), rb = idx >> 2;
    const int r = lane & 31, hh = lane >> 5;
    {
      const unsigned l16 = lane * 16;
      const unsigned ldsb = (unsigned)(size_t)lds;
      auto dma = [&](const char* sbase, unsigned dst) {
        asm volatile("s_mov_b32 m0, %0\n\ts_nop 0\n\tglobal_load_lds_dwordx4 %1, %2" ::"s"(dst), "v"(l16), "s"(sbase) : "memory", "m0");
      };
      const char* qb_ = ws + OFF_KQ + (size_t)((head * 5) >> 1) * 32768 + rb * 4096 + (w & 3) * 1024;
      dma(qb_ + (w >> 2) * 32768, ldsb + K2_Q + w * 1024);
      dma(qb_ + 2 * 32768, ldsb + K2_Q + (8 + (w & 3)) * 1024);
      const char* kb_ = ws + OFF_KQ + (size_t)(80 + ((head * 5) >> 1)) * 32768 + rb * 4096 - 4096 + w * 1024;
      dma(kb_, ldsb + K2_K + w * 1024);
      dma(kb_ + 32768, ldsb + K2_K + (8 + w) * 1024);
      dma(kb_ + 65536, ldsb + K2_K + (16 + w) * 1024);
      const char* vb_ = ws + OFF_VT + (size_t)head * VT_HEAD + (16 * rb - 16) * VT_TG;
      const int j3 = (w + 16 <= 20) ? (w + 16) : w;
      dma(vb_ + w * 1024, ldsb + K2_V + w * 1024);
      dma(vb_ + (w + 8) * 1024, ldsb + K2_V + (w + 8) * 1024);
      dma(vb_ + j3 * 1024, ldsb + K2_V + j3 * 1024);
    }
    f32x16 mk0, mk8;
#pragma unroll
    for (int j = 0; j < 16; ++j) {
      const int kl = (j & 3) + 8 * (j >> 2) + 4 * hh;
      mk0[j] = (kl > r) ? 0.f : -INFINITY;
      mk8[j] = (kl <= r) ? 0.f : -INFINITY;
    }
    asm volatile("" : "+v"(mk0), "+v"(mk8));
    asm volatile("s_waitcnt vmcnt(3)" ::: "memory");
    if (lane == 0) __hip_atomic_fetch_add(flg, 1u, __ATOMIC_RELAXED, __HIP_MEMORY_SCOPE_WORKGROUP);
    for (int sp = 0; sp < (1 << 22); ++sp) {
      if (__hip_atomic_load(flg, __ATOMIC_RELAXED, __HIP_MEMORY_SCOPE_WORKGROUP) >= 8u) break;
      __builtin_amdgcn_s_sleep(1);
    }
    asm volatile("" ::: "memory");
    const int dcl = (r < 20) ? r : 20;
    const int qt = w;
    const int tmin = (rb == 0) ? (8 - qt) : 0;
    bf16x8 qa, qb;
    {
      LAS char* qp = lds + K2_Q + (32 * qt + r) * 16;
      qa = *(LAS bf16x8*)(qp + (hh + (head & 1)) * 4096);
      u32x2 c = *(LAS u32x2*)(qp + ((head & 1) ? 8 : 2 * 4096));
      u32x4 t1_ = {hh ? 0u : c[0], hh ? 0u : c[1], 0u, 0u};
      qb = __builtin_bit_cast(bf16x8, t1_);
    }
    LAS char* kp = lds + K2_K + (32 * qt + r) * 16;
    LAS char* kpa = kp + (hh + (head & 1)) * 8192;
    LAS char* kpc = kp + ((head & 1) ? 8 : 2 * 8192);
    bf16x8 ka[3], kc[3], vf[3][2];
    auto rdk = [&](int t) {
      ka[t % 3] = *(LAS bf16x8*)(kpa + t * 512);
      u32x2 c_ = *(LAS u32x2*)(kpc + t * 512);
      kc[t % 3] = __builtin_bit_cast(bf16x8, u32x4{c_[0], c_[1], 0u, 0u});
    };
    f32x16 sa, sb;
    float nm;
    {
      f32x16 z;
#pragma unroll
      for (int j = 0; j < 16; ++j) z[j] = 0.f;
      rdk(8); rdk(7);
      float mx[4];
      __builtin_amdgcn_sched_barrier(0);
#pragma unroll
      for (int t = 8; t >= -1; --t) {
        if (t >= 0) {
          f32x16 c0 = (t == 8) ? mk8 : ((t == 0) ? mk0 : z);
          f32x16 v = MFMA32(ka[t % 3], qa, c0);
          if (t - 2 >= 0) rdk(t - 2);
          v = MFMA32(kc[t % 3], qb, v);
          if (t & 1) sb = v; else sa = v;
        }
        if (t < 8) {
          const f32x16& p = ((t + 1) & 1) ? sb : sa;
          if (t + 1 == 8) {
#pragma unroll
            for (int u = 0; u < 4; ++u) mx[u] = fmaxf(fmaxf(p[4 * u], p[4 * u + 1]), fmaxf(p[4 * u + 2], p[4 * u + 3]));
          } else if (t + 1 >= tmin) {
#pragma unroll
            for (int j = 0; j < 16; j += 8)
#pragma unroll
              for (int u = 0; u < 4; ++u) mx[u] = fmaxf(fmaxf(mx[u], p[j + 2 * u]), p[j + 2 * u + 1]);
          }
          asm volatile("" : "+v"(mx[0]), "+v"(mx[1]), "+v"(mx[2]), "+v"(mx[3]));
        }
        __builtin_amdgcn_sched_barrier(0);
      }
      nm = -swap_max(fmaxf(fmaxf(mx[0], mx[1]), fmaxf(mx[2], mx[3])));
    }
    asm volatile("s_waitcnt vmcnt(0) lgkmcnt(0)" ::: "memory");
    {
      const int o1 = (lane >> 1) * VT_TG + (lane & 1) * 336 + 320;
      if (((o1 >> 10) & 7) == w) {
        u32x4 one = {0x3f803f80u, 0x3f803f80u, 0x3f803f80u, 0x3f803f80u};
        *(LAS u32x4*)(lds + K2_V + o1) = one;
      }
      asm volatile("s_waitcnt lgkmcnt(0)" ::: "memory");
    }
    if (lane == 0) __hip_atomic_fetch_add(flg + 2, 1u, __ATOMIC_RELAXED, __HIP_MEMORY_SCOPE_WORKGROUP);
    rdk(8); rdk(7);
    f32x16 cm;
#pragma unroll
    for (int j = 0; j < 16; ++j) { cm[j] = nm; mk0[j] += nm; mk8[j] += nm; }
    for (int sp = 0; sp < (1 << 22); ++sp) {
      if (__hip_atomic_load(flg + 2, __ATOMIC_RELAXED, __HIP_MEMORY_SCOPE_WORKGROUP) >= 8u) break;
      __builtin_amdgcn_s_sleep(1);
    }
    asm volatile("" ::: "memory");
    {
      LAS char* vp = lds + K2_V + (2 * qt) * VT_TG + dcl * 16 + hh * 336;
      auto rdv = [&](int t) {
        vf[t % 3][0] = *(LAS bf16x8*)(vp + (2 * t) * VT_TG);
        vf[t % 3][1] = *(LAS bf16x8*)(vp + (2 * t + 1) * VT_TG);
      };
      rdv(8);
      f32x16 o;
#pragma unroll
      for (int j = 0; j < 16; ++j) o[j] = 0.f;
      u32x4 pp[2];
      __builtin_amdgcn_sched_barrier(0);
#pragma unroll
      for (int t = 8; t >= -2; --t) {
        const bool ex = (t + 1 <= 8 && t + 1 >= 0);
        f32x16& p = ((t + 1) & 1) ? sb : sa;
        f32x16& v = (t & 1) ? sb : sa;
        auto exps = [&](int k) {
          if (ex) {
#pragma unroll
            for (int j = 4 * k; j < 4 * k + 4; ++j) p[j] = __builtin_amdgcn_exp2f(p[j]);
            asm volatile("" : "+v"(p));
          }
          __builtin_amdgcn_sched_barrier(0);
        };
        const bool pv = (t + 2 <= 8) && (t + 2 >= tmin);
        if (t + 2 <= 8) { if (pv) o = MFMA32(vf[(t + 2) % 3][0], __builtin_bit_cast(bf16x8, pp[0]), o); }
        exps(0);
        if (t + 2 <= 8) { if (pv) o = MFMA32(vf[(t + 2) % 3][1], __builtin_bit_cast(bf16x8, pp[1]), o); }
        if (t - 1 >= 0) rdv(t - 1);
        exps(1);
        if (t >= 0) {
          f32x16 c0 = (t == 8) ? mk8 : ((t == 0) ? mk0 : cm);
          v = MFMA32(ka[t % 3], qa, c0);
        }
        exps(2);
        if (t >= 0) {
          v = MFMA32(kc[t % 3], qb, v);
          if (t - 2 >= 0) rdk(t - 2);
        }
        exps(3);
        if (ex) {
#pragma unroll
          for (int s = 0; s < 2; ++s)
#pragma unroll
            for (int j = 0; j < 4; ++j) pp[s][j] = pk2(p[8 * s + 2 * j], p[8 * s + 2 * j + 1]);
          asm volatile("" : "+v"(pp[0]), "+v"(pp[1]));
        }
        __builtin_amdgcn_sched_barrier(0);
      }
      const float rl = __builtin_amdgcn_rcpf(o[12]);
      const int tok = 256 * rb + 32 * qt + r;
      char* ob = ws + OFF_OT + ((size_t)(tok >> 4) * 20 * 64 + (tok & 15)) * 16;
#pragma unroll
      for (int a = 0; a < 3; ++a) {
        const int k = head * 20 + 8 * a + 4 * hh;
        unsigned long long v = (unsigned long long)pk2(o[4 * a] * rl, o[4 * a + 1] * rl) |
                               ((unsigned long long)pk2(o[4 * a + 2] * rl, o[4 * a + 3] * rl) << 32);
        char* p = ob + (size_t)(k >> 5) * 1024 + ((k >> 3) & 3) * 256 + (k & 7) * 2;
        if (a < 2 || hh == 0)
          __hip_atomic_store((unsigned long long*)p, v, __ATOMIC_RELAXED, __HIP_MEMORY_SCOPE_AGENT);
      }
    }
    asm volatile("s_waitcnt vmcnt(0)" ::: "memory");
    if (lane == 0) {
      unsigned old = __hip_atomic_fetch_add(flg + 1, 1u, __ATOMIC_RELAXED, __HIP_MEMORY_SCOPE_WORKGROUP);
      if (old == 7u)
        __hip_atomic_fetch_add((unsigned*)(ws + OFF_CNT + rb * 256), 1u, __ATOMIC_RELAXED, __HIP_MEMORY_SCOPE_AGENT);
    }
    return;
  }

  const int kw = w - 8;
  const int rb64 = 8 * (xcd >> 1) + (idx & 7), ct = 4 * (xcd & 1) + (idx >> 3);
  const char* wp = ws + OFF_W2 + (size_t)ct * 68 * 5 * 1024 + lane * 16;
  const char* hp = ws + OFF_HT + (size_t)(4 * rb64) * 48 * 1024 + lane * 16;
  auto orsrc = __builtin_amdgcn_make_buffer_rsrc((void*)(ws + OFF_OT), 0, (int)SZ_OT, 0x00020000);
  const int ovoff = (4 * rb64) * 20 * 1024 + lane * 16;
  f32x4 acc[5][4];
#pragma unroll
  for (int n = 0; n < 5; ++n)
#pragma unroll
    for (int i = 0; i < 4; ++i) acc[n][i] = f32x4{0.f, 0.f, 0.f, 0.f};
  constexpr int GD = 2;
  bf16x8 bw[GD][5], bt[GD][4];
  auto loadT = [&](int st, int sl) {
    if (st < 12) {
      const int ks = 12 * kw + st;
#pragma unroll
      for (int i = 0; i < 4; ++i) bt[sl][i] = *(const bf16x8*)(hp + (size_t)(i * 48 + ks) * 1024);
    } else {
      const int ko = 5 * kw + (st - 12);
#pragma unroll
      for (int i = 0; i < 4; ++i)
        bt[sl][i] = __builtin_bit_cast(bf16x8, __builtin_amdgcn_raw_buffer_load_b128(orsrc, ovoff + (i * 20 + ko) * 1024, 0, 16));
    }
    __builtin_amdgcn_sched_barrier(0);
  };
  auto loadW = [&](int st, int sl) {
    const int ks = (st < 12) ? (12 * kw + st) : (48 + 5 * kw + (st - 12));
#pragma unroll
    for (int n = 0; n < 5; ++n) bw[sl][n] = *(const bf16x8*)(wp + (size_t)(ks * 5 + n) * 1024);
    __builtin_amdgcn_sched_barrier(0);
  };
  auto mm = [&](int st) {
#pragma unroll
    for (int n = 0; n < 5; ++n)
#pragma unroll
      for (int i = 0; i < 4; ++i) acc[n][i] = MFMA16(bw[st % GD][n], bt[st % GD][i], acc[n][i]);
    __builtin_amdgcn_sched_barrier(0);
  };
  __builtin_amdgcn_s_sleep(GSLEEP);
#pragma unroll
  for (int st = 0; st < GD; ++st) { loadT(st, st); loadW(st, st); }
#pragma unroll
  for (int st = 0; st < 12; ++st) {
    mm(st);
    if (st + GD < 12) loadT(st + GD, st % GD);
    loadW(st + GD, st % GD);
  }
  {
    if (kw == 0) {
      const unsigned* cnt = (const unsigned*)(ws + OFF_CNT + (rb64 >> 2) * 256);
      for (int sp = 0; sp < (1 << 22); ++sp) {
        if (__hip_atomic_load(cnt, __ATOMIC_RELAXED, __HIP_MEMORY_SCOPE_AGENT) >= 32u) break;
        __builtin_amdgcn_s_sleep(PSLEEP);
      }
      __hip_atomic_store(flg + 3, 1u, __ATOMIC_RELAXED, __HIP_MEMORY_SCOPE_WORKGROUP);
    } else {
      for (int sp = 0; sp < (1 << 24); ++sp) {
        if (__hip_atomic_load(flg + 3, __ATOMIC_RELAXED, __HIP_MEMORY_SCOPE_WORKGROUP) != 0u) break;
        __builtin_amdgcn_s_sleep(1);
      }
    }
    __builtin_amdgcn_fence(__ATOMIC_ACQUIRE, "wavefront");
    asm volatile("" ::: "memory");
  }
  loadT(12, 0); loadT(13, 1);
  mm(12); loadT(14, 0); loadW(14, 0);
  mm(13); loadT(15, 1); loadW(15, 1);
  mm(14); loadT(16, 0); loadW(16, 0);
  mm(15);
  mm(16);
  const int tq = tid - 512;
  u32x4 xv[3];
#pragma unroll
  for (int it = 0; it < 3; ++it) {
    const int item = tq + 256 * it;
    const int tk = item / 10, n8 = item - tk * 10;
    if (item < 640) xv[it] = *(const u32x4*)(x + (size_t)(64 * rb64 + tk) * 1280 + (80 * ct + 8 * n8) * 2);
  }
  {
    LAS char* rp = lds + K2_RED + (kw * 64 + (lane & 15)) * RED_ROW + (lane >> 4) * 16;
#pragma unroll
    for (int n = 0; n < 5; ++n)
#pragma unroll
      for (int i = 0; i < 4; ++i) *(LAS f32x4*)(rp + i * 16 * RED_ROW + n * 64) = acc[n][i];
  }
  asm volatile("s_waitcnt lgkmcnt(0)\n\ts_barrier" ::: "memory");
#pragma unroll
  for (int it = 0; it < 3; ++it) {
    const int item = tq + 256 * it;
    const int tk = item / 10, n8 = item - tk * 10;
    if (item < 640) {
      LAS char* rp = lds + K2_RED + tk * RED_ROW + n8 * 32;
      f32x4 a = *(LAS f32x4*)rp, c = *(LAS f32x4*)(rp + 16);
#pragma unroll
      for (int k = 1; k < 4; ++k) {
        a += *(LAS f32x4*)(rp + k * 64 * RED_ROW);
        c += *(LAS f32x4*)(rp + k * 64 * RED_ROW + 16);
      }
      u32x4 xx = xv[it];
      u32x4 v;
      v[0] = pk2(a[0] + __uint_as_float(xx[0] << 16), a[1] + __uint_as_float(xx[0] & 0xffff0000u));
      v[1] = pk2(a[2] + __uint_as_float(xx[1] << 16), a[3] + __uint_as_float(xx[1] & 0xffff0000u));
      v[2] = pk2(c[0] + __uint_as_float(xx[2] << 16), c[1] + __uint_as_float(xx[2] & 0xffff0000u));
      v[3] = pk2(c[2] + __uint_as_float(xx[3] << 16), c[3] + __uint_as_float(xx[3] & 0xffff0000u));
      *(u32x4*)(out + (size_t)(64 * rb64 + tk) * 1280 + (80 * ct + 8 * n8) * 2) = v;
    }
  }
}

}

extern "C" void kernel_launch(void* const* d_in, const int* in_sizes, int n_in, void* d_out, int out_size,
                              void* d_ws, size_t ws_size, hipStream_t stream) {
  const char* x = (const char*)d_in[0];
  const char* wqkv = (const char*)d_in[1];
  const char* wo = (const char*)d_in[2];
  const char* wg = (const char*)d_in[4];
  const char* wu = (const char*)d_in[5];
  const char* wd = (const char*)d_in[6];
  k1_kernel<<<256, 512, 0, stream>>>(x, wqkv, wo, wg, wu, wd, (char*)d_ws);
  k2_kernel<<<256, 768, 0, stream>>>(x, (char*)d_ws, (char*)d_out);
}
```

```cpp
#include <hip/hip_runtime.h>
#include <hip/hip_bf16.h>
#include <stdint.h>

typedef short bf16x8 __attribute__((ext_vector_type(8)));
typedef short s16x4 __attribute__((ext_vector_type(4)));
typedef float f32x16 __attribute__((ext_vector_type(16)));
typedef float f32x4 __attribute__((ext_vector_type(4)));
typedef float f32x2 __attribute__((ext_vector_type(2)));
typedef unsigned u32x4 __attribute__((ext_vector_type(4)));
typedef unsigned u32x2 __attribute__((ext_vector_type(2)));
typedef __bf16 bf2 __attribute__((ext_vector_type(2)));
#define LAS __attribute__((address_space(3)))
#define DI __device__ __forceinline__

namespace {

constexpr float QSCALE = 0.22360679774997896f * 1.4426950408889634f;

constexpr size_t OFF_CNT = 0;
constexpr size_t OFF_KQ = 32768;
constexpr size_t SZ_KQ = 160ull * 2048 * 16;
constexpr size_t OFF_VT = OFF_KQ + SZ_KQ;
constexpr int VT_TG = 21 * 32;
constexpr int VT_HEAD = 128 * VT_TG;
constexpr size_t SZ_VT = 32ull * VT_HEAD;
constexpr size_t OFF_HT = OFF_VT + SZ_VT;
constexpr size_t SZ_HT = 128ull * 48 * 1024;
constexpr size_t OFF_OT = OFF_HT + SZ_HT;
constexpr size_t SZ_OT = 128ull * 20 * 1024;
constexpr size_t OFF_W2 = OFF_OT + SZ_OT;

DI float swap_sum(float x) {
  auto r = __builtin_amdgcn_permlane32_swap(__float_as_uint(x), __float_as_uint(x), false, false);
  return __uint_as_float(r[0]) + __uint_as_float(r[1]);
}
DI float swap_max(float x) {
  auto r = __builtin_amdgcn_permlane32_swap(__float_as_uint(x), __float_as_uint(x), false, false);
  return fmaxf(__uint_as_float(r[0]), __uint_as_float(r[1]));
}
DI unsigned pk2(float a, float b) {
  f32x2 v = {a, b};
  return __builtin_bit_cast(unsigned, __builtin_convertvector(v, bf2));
}
DI s16x4 tr_read(LAS char* p) {
  return __builtin_amdgcn_ds_read_tr16_b64_v4i16((LAS s16x4*)p);
}
template <int N> DI void wait_bar() {
  asm volatile("s_waitcnt vmcnt(%0) lgkmcnt(0)\n\ts_barrier" ::"i"(N) : "memory");
}
#define MFMA32(a, b, c) __builtin_amdgcn_mfma_f32_32x32x16_bf16((a), (b), (c), 0, 0, 0)
#define MFMA16(a, b, c) __builtin_amdgcn_mfma_f32_16x16x32_bf16((a), (b), (c), 0, 0, 0)

constexpr int K1_WSLOT = 20480, K1_NWS = 5;
constexpr int K1_XOFF = K1_WSLOT * K1_NWS;
constexpr int K1_XSLOT = 16384, K1_NXS = 3;
constexpr int K1_CS = K1_XOFF + K1_XSLOT * K1_NXS;
constexpr int K1_ST = K1_CS + 2560;
constexpr int K1_LDS = K1_ST + 1024;

template <int NL> DI void warm_code(int lane) {
  const char* pc;
  asm volatile("s_getpc_b64 %0" : "=s"(pc));
  unsigned d = 0;
  const unsigned off = lane * 128;
#pragma unroll
  for (int i = 0; i < NL; ++i) asm volatile("global_load_dword %0, %1, %2" : "+v"(d) : "v"(off), "s"(pc + i * 8000) : "memory");
  asm volatile("s_waitcnt vmcnt(0)" : "+v"(d) :: "memory");
}
DI float bf_lo(unsigned v) { return __uint_as_float(v << 16); }
DI float bf_hi(unsigned v) { return __uint_as_float(v & 0xffff0000u); }
DI void bar_only() { asm volatile("s_barrier" ::: "memory"); }

DI void st_ws(__amdgpu_buffer_rsrc_t rs, unsigned off, u32x4 v) { __builtin_amdgcn_raw_buffer_store_b128(v, rs, (int)off, 0, 0); }
DI void st_ws(__amdgpu_buffer_rsrc_t rs, unsigned off, u32x2 v) { __builtin_amdgcn_raw_buffer_store_b64(v, rs, (int)off, 0, 0); }

template <int TYPE>
DI void k1_body(const char* __restrict__ x, const char* __restrict__ wqkv, const char* __restrict__ wg,
                const char* __restrict__ wu, const char* __restrict__ wd, const char* __restrict__ wo,
                char* __restrict__ ws, LAS char* lds, int rb, int nt) {
  constexpr int NU = (TYPE == 1) ? 4 : 5;
  const int tid = threadIdx.x, lane = tid & 63;
  const int w = __builtin_amdgcn_readfirstlane(tid >> 6);
  const int qcol0 = (TYPE == 0) ? nt * 64 : (TYPE == 1) ? (12 + 4 * (nt - 12)) * 64 : 1280 + 5 * (nt - 14) * 64;

  if (w >= 4) {
    const int hw = w - 4;
    const char* ub[5];
    if (TYPE == 0) {
      ub[0] = wg + (2 * nt) * 128; ub[1] = wu + (2 * nt) * 128;
      ub[2] = wg + (2 * nt + 1) * 128; ub[3] = wu + (2 * nt + 1) * 128;
      ub[4] = wqkv + qcol0 * 2;
    } else {
      for (int u = 0; u < 5; ++u) ub[u] = wqkv + (qcol0 + 64 * u) * 2;
    }
    unsigned voffA, voffB, xvoff0, xvoff1;
    {
      int row = 8 * hw + (lane >> 3);
      int cc = ((((lane >> 2) & 1) ^ ((row >> 1) & 1)) << 2) + (lane & 3);
      voffA = row * 3072 + cc * 16;
      voffB = row * 3840 + cc * 16;
      int xr = rb * 128 + 32 * hw + (lane >> 3);
      int sc = (lane & 7) ^ (lane >> 4);
      xvoff0 = xr * 1280 + sc * 16;
      xvoff1 = xr * 1280 + (sc ^ 4) * 16;
    }
    const unsigned ldsb = (unsigned)(size_t)lds;
    auto dma = [&](const char* sbase, unsigned voff, unsigned dst) {
      asm volatile("s_mov_b32 m0, %0\n\ts_nop 0\n\tglobal_load_lds_dwordx4 %1, %2" ::"s"(dst), "v"(voff), "s"(sbase) : "memory", "m0");
    };
    const int coff = (blockIdx.x & 1) * 5 + ((blockIdx.x >> 1) & 3);
    auto issueW = [&](int slot) {
      const unsigned dst = ldsb + (slot % K1_NWS) * 4096 + hw * 1024;
      int gs = slot + 2 * coff; if (gs >= 20) gs -= 20;
#pragma unroll
      for (int u = 0; u < NU; ++u) {
        const bool a = (TYPE == 0 && u < 4);
        dma(ub[u] + (size_t)gs * 32 * (a ? 3072 : 3840), a ? voffA : voffB, dst + u * K1_WSLOT);
      }
    };
    auto issueX = [&](int c) {
      const unsigned dst = ldsb + K1_XOFF + (c % K1_NXS) * K1_XSLOT + hw * 4096;
      int gc = c + coff; if (gc >= 10) gc -= 10;
#pragma unroll
      for (int i = 0; i < 4; ++i) dma(x + gc * 128 + i * 8 * 1280, (i & 1) ? xvoff1 : xvoff0, dst + i * 1024);
    };
    issueW(0); issueX(0); issueW(1); issueW(2); issueX(1);
    wait_bar<2 * NU + 4>();
    issueW(3);
#pragma unroll
    for (int i = 0; i < 19; ++i) {
      const int nw = ((i + 3 < 19) ? (i + 3) : 19) - (i + 1);
      const bool hx = (i & 1) ? (i <= 15) : (i <= 16);
      const int cnt = NU * nw + (hx ? 4 : 0);
      if (cnt == 2 * NU + 4) wait_bar<2 * NU + 4>();
      else if (cnt == 2 * NU) wait_bar<2 * NU>();
      else if (cnt == NU) wait_bar<NU>();
      else wait_bar<0>();
      if (i + 4 < 20) issueW(i + 4);
      if ((i & 1) == 0 && (i / 2 + 2) < 10) issueX(i / 2 + 2);
    }
    bar_only();
    {
      const int xcd = blockIdx.x & 7, ks = (blockIdx.x >> 3) + 32 * hw;
      if (hw < 3 && ks < 68) {
        const char* src = (ks < 48) ? (wd + (size_t)(32 * ks) * 1280) : (wo + (size_t)(32 * (ks - 48)) * 1280);
        src += xcd * 160;
        const unsigned scr = hw * 5120;
#pragma unroll
        for (int j = 0; j < 5; ++j) {
          const int sl = j * 64 + lane, row = sl / 10, cc = sl - row * 10;
          dma(src, row * 1280 + cc * 16, ldsb + scr + j * 1024);
        }
        asm volatile("s_waitcnt vmcnt(0)" ::: "memory");
        LAS char* tp = lds + scr + (8 * (lane >> 4) + ((lane & 15) >> 2)) * 160 + 8 * (lane & 3);
        char* dst = ws + OFF_W2 + (size_t)((xcd * 68 + ks) * 5) * 1024 + lane * 16;
#pragma unroll
        for (int n = 0; n < 5; ++n) {
          s16x4 lo = tr_read(tp + n * 32), hi = tr_read(tp + n * 32 + 640);
          *(bf16x8*)(dst + n * 1024) = __builtin_shufflevector(lo, hi, 0, 1, 2, 3, 4, 5, 6, 7);
        }
      }
    }
    return;
  }

  const int tw = w & 1, ch = w >> 1;
  const int r = lane & 31, hh = lane >> 5;
  int xa[2][2];
  {
    int e = hh ^ ((r >> 1) & 7);
#pragma unroll
    for (int g = 0; g < 2; ++g)
#pragma unroll
      for (int ph = 0; ph < 2; ++ph) {
        int tg = 2 * tw + (g ^ ch), s = ph ^ tw;
        xa[g][ph] = K1_XOFF + (32 * tg + r) * 128 + (((2 * s) ^ e) << 4);
      }
  }
  int wa[2][3];
  {
    int G = lane >> 4, chh = G & 1, q = (lane & 15) >> 2, p = lane & 3;
#pragma unroll
    for (int ph = 0; ph < 2; ++ph) {
      int b0 = (8 * hh + q) * 128 + 32 * chh + 8 * p + (ph ^ tw) * 2048;
      wa[ph][0] = b0 + 64 * (0 ^ (q >> 1)) + ch * 2 * K1_WSLOT;
      wa[ph][1] = b0 + 64 * (1 ^ (q >> 1)) + ch * 2 * K1_WSLOT;
      wa[ph][2] = b0 + 64 * (ch ^ (q >> 1)) + 4 * K1_WSLOT;
    }
  }
  f32x16 acc[2][5];
#pragma unroll
  for (int g = 0; g < 2; ++g)
#pragma unroll
    for (int i = 0; i < 5; ++i)
#pragma unroll
      for (int j = 0; j < 16; ++j) acc[g][i][j] = 0.f;
  f32x4 cs[5];
#pragma unroll
  for (int i = 0; i < 5; ++i) cs[i] = f32x4{0.f, 0.f, 0.f, 0.f};
  bf16x8 onep;
  {
    const unsigned o = ((lane & 15) == ((lane >> 4) & 1)) ? 0x3f803f80u : 0u;
    u32x4 t = {o, o, o, o};
    onep = __builtin_bit_cast(bf16x8, t);
  }
  float s0 = 0.f, q0 = 0.f;

  bf16x8 xf[2];
  s16x4 wl[5], wh[5];
  auto rdx = [&](int i, int ph, int g) {
    LAS char* xb = lds + ((i / 2) % K1_NXS) * K1_XSLOT;
    xf[g] = *(LAS bf16x8*)(xb + (xa[g][ph] ^ ((i & 1) * 64)));
  };
  auto rdw = [&](int i, int ph, int c5) {
    LAS char* p = lds + (i % K1_NWS) * 4096 + ((c5 < 4) ? ((c5 >> 1) * K1_WSLOT + wa[ph][c5 & 1]) : wa[ph][2]);
    wl[c5] = tr_read(p);
    wh[c5] = tr_read(p + 512);
  };
  auto phase = [&](int i, int ph, int ni, int nph, bool do_rd) {
#pragma unroll
    for (int k = 0; k < 2 * NU; ++k) {
      const int g = (k >= NU) ? 1 : 0, c5 = k - g * NU;
      bf16x8 wfr = __builtin_shufflevector(wl[c5], wh[c5], 0, 1, 2, 3, 4, 5, 6, 7);
      if (TYPE == 2) acc[g][c5] = MFMA32(xf[g], wfr, acc[g][c5]);
      else acc[g][c5] = MFMA32(wfr, xf[g], acc[g][c5]);
      if (ph == 0 && g == 0) cs[c5] = MFMA16(wfr, onep, cs[c5]);
      if (k == 0) rdx(i, ph, 1);
      if (do_rd && g == 1) {
        if (c5 == 0) rdx(ni, nph, 0);
        rdw(ni, nph, c5);
      }
      if (k < 4) {
        u32x4 xu = __builtin_bit_cast(u32x4, xf[0]);
        float l = bf_lo(xu[k]), h = bf_hi(xu[k]);
        s0 += l; q0 = fmaf(l, l, q0); s0 += h; q0 = fmaf(h, h, q0);
        asm volatile("" : "+v"(s0), "+v"(q0));
      }
      __builtin_amdgcn_sched_barrier(0);
    }
  };

  bar_only();
  rdx(0, 0, 0);
#pragma unroll
  for (int c5 = 0; c5 < NU; ++c5) rdw(0, 0, c5);
  __builtin_amdgcn_sched_barrier(0);
#pragma unroll
  for (int i = 0; i < 20; ++i) {
    phase(i, 0, i, 1, true);
    if (i < 19) {
      bar_only();
      __builtin_amdgcn_sched_barrier(0);
      phase(i, 1, i + 1, 0, true);
    } else {
      phase(i, 1, 0, 0, false);
    }
  }

  int tz = threadIdx.x;
  asm volatile("" : "+v"(tz));
  const int lane_ = tz & 63, r_ = tz & 31, hh_ = (tz >> 5) & 1;
#define lane lane_
#define r r_
#define hh hh_
  LAS float* csb = (LAS float*)(lds + K1_CS) + ch * 160;
  const __amdgpu_buffer_rsrc_t wsr = __builtin_amdgcn_make_buffer_rsrc(ws, 0, 0x7fffffff, 0x00020000);
  {
    const float ssum = swap_sum(s0), ssq = swap_sum(q0);
    const float mean = ssum * (1.f / 640.f);
    const float var = fmaxf(ssq * (1.f / 640.f) - mean * mean, 0.f);
    const float rs = __builtin_amdgcn_rsqf(var + 1e-5f);
    if (lane < 32) {
      f32x2 st = {-mean, rs};
      *(LAS f32x2*)(lds + K1_ST + (32 * (2 * tw + ch) + lane) * 8) = st;
    }
    if ((lane & 15) < 2) {
      LAS float* cp = csb + tw * 320 + 16 * (lane & 15) + 4 * (lane >> 4);
#pragma unroll
      for (int c5 = 0; c5 < NU; ++c5) *(LAS f32x4*)(cp + c5 * 32) = cs[c5];
    }
  }
  asm volatile("s_waitcnt lgkmcnt(0)\n\ts_barrier" ::: "memory");

  if (TYPE != 2) {
    f32x2 st[2];
#pragma unroll
    for (int g = 0; g < 2; ++g) st[g] = *(LAS f32x2*)(lds + K1_ST + (32 * (2 * tw + (g ^ ch)) + r) * 8);
#pragma unroll
    for (int c5 = 0; c5 < NU; ++c5) {
      f32x4 c4[4];
#pragma unroll
      for (int q4 = 0; q4 < 4; ++q4) c4[q4] = *(LAS f32x4*)(csb + c5 * 32 + 8 * q4 + 4 * hh) + *(LAS f32x4*)(csb + 320 + c5 * 32 + 8 * q4 + 4 * hh);
#pragma unroll
      for (int g = 0; g < 2; ++g) {
        const f32x2 nm2 = {st[g][0], st[g][0]};
#pragma unroll
        for (int q4 = 0; q4 < 4; ++q4)
#pragma unroll
          for (int j = 0; j < 2; ++j) {
            f32x2 v = {acc[g][c5][4 * q4 + 2 * j], acc[g][c5][4 * q4 + 2 * j + 1]};
            v = nm2 * f32x2{c4[q4][2 * j], c4[q4][2 * j + 1]} + v;
            acc[g][c5][4 * q4 + 2 * j] = v[0]; acc[g][c5][4 * q4 + 2 * j + 1] = v[1];
          }
      }
    }
#pragma unroll
    for (int g = 0; g < 2; ++g) {
      const int tok = rb * 128 + 32 * (2 * tw + (g ^ ch)) + r;
      const float rs = st[g][1];
      auto store_qk = [&](const f32x16& a, int nb) {
        const unsigned kq = (unsigned)OFF_KQ + tok * 16 + (nb / 8 + hh) * 32768;
        const float rsq = (nb < 640) ? rs * QSCALE : rs;
        const f32x2 rs2 = {rsq, rsq};
        unsigned P[8];
#pragma unroll
        for (int j = 0; j < 8; ++j) {
          f32x2 v = f32x2{a[2 * j], a[2 * j + 1]} * rs2;
          P[j] = pk2(v[0], v[1]);
        }
#pragma unroll
        for (int qp = 0; qp < 2; ++qp) {
          auto r0 = __builtin_amdgcn_permlane32_swap(P[4 * qp], P[4 * qp + 2], false, false);
          auto r1 = __builtin_amdgcn_permlane32_swap(P[4 * qp + 1], P[4 * qp + 3], false, false);
          u32x4 v = {r0[0], r1[0], r0[1], r1[1]};
          st_ws(wsr, kq + qp * 65536, v);
        }
      };
      if (TYPE == 0) {
        store_qk(acc[g][4], qcol0 + 32 * ch);
        const unsigned ht = (unsigned)OFF_HT + ((tok >> 4) * 48 * 64 + (tok & 15) + 16 * hh) * 16 + (2 * (2 * nt + ch)) * 1024;
        const float k1 = -1.4426950408889634f * rs;
        const f32x2 k12 = {k1, k1};
        const float irs2 = __builtin_amdgcn_rcpf(rs * rs);
        const f32x2 ir2 = {irs2, irs2};
#pragma unroll
        for (int c = 0; c < 2; ++c) {
          unsigned P[8];
#pragma unroll
          for (int j = 0; j < 8; ++j) {
            f32x2 gg = {acc[g][c][2 * j], acc[g][c][2 * j + 1]};
            f32x2 uu = {acc[g][2 + c][2 * j], acc[g][2 + c][2 * j + 1]};
            f32x2 t = gg * k12;
            f32x2 e = {__builtin_amdgcn_exp2f(t[0]), __builtin_amdgcn_exp2f(t[1])};
            f32x2 d = e * ir2 + ir2;
            f32x2 rr = {__builtin_amdgcn_rcpf(d[0]), __builtin_amdgcn_rcpf(d[1])};
            f32x2 hv = (gg * uu) * rr;
            P[j] = pk2(hv[0], hv[1]);
          }
#pragma unroll
          for (int qp = 0; qp < 2; ++qp) {
            auto r0 = __builtin_amdgcn_permlane32_swap(P[4 * qp], P[4 * qp + 2], false, false);
            auto r1 = __builtin_amdgcn_permlane32_swap(P[4 * qp + 1], P[4 * qp + 3], false, false);
            u32x4 v = {r0[0], r1[0], r0[1], r1[1]};
            st_ws(wsr, ht + c * 1024 + qp * 512, v);
          }
        }
      } else {
#pragma unroll
        for (int c5 = 0; c5 < 4; ++c5) store_qk(acc[g][c5], qcol0 + 128 * ch + 32 * c5);
      }
    }
  } else {
    int voff[5];
    float cn[5];
#pragma unroll
    for (int c5 = 0; c5 < 5; ++c5) {
      const int nv = qcol0 - 1280 + ((c5 < 4) ? (128 * ch + 32 * c5) : (256 + 32 * ch)) + r;
      const int head = nv / 20, d = nv - head * 20;
      voff[c5] = head * VT_HEAD + hh * 336 + d * 16;
      cn[c5] = csb[c5 * 32 + r] + csb[320 + c5 * 32 + r];
    }
#pragma unroll
    for (int g = 0; g < 2; ++g) {
      const int tb = 32 * (2 * tw + (g ^ ch));
      f32x2 mm[8], rr[8];
#pragma unroll
      for (int q4 = 0; q4 < 4; ++q4) {
        f32x4 a = *(LAS f32x4*)(lds + K1_ST + (tb + 8 * q4 + 4 * hh) * 8);
        f32x4 b = *(LAS f32x4*)(lds + K1_ST + (tb + 8 * q4 + 4 * hh) * 8 + 16);
        mm[2 * q4] = f32x2{a[0], a[2]}; rr[2 * q4] = f32x2{a[1], a[3]};
        mm[2 * q4 + 1] = f32x2{b[0], b[2]}; rr[2 * q4 + 1] = f32x2{b[1], b[3]};
      }
      const unsigned vb = (unsigned)OFF_VT + ((rb * 128 + tb) >> 4) * VT_TG;
#pragma unroll
      for (int c5 = 0; c5 < 5; ++c5) {
        const f32x2 c2 = {cn[c5], cn[c5]};
#pragma unroll
        for (int s = 0; s < 2; ++s) {
          u32x4 v;
#pragma unroll
          for (int j = 0; j < 4; ++j) {
            f32x2 t = {acc[g][c5][8 * s + 2 * j], acc[g][c5][8 * s + 2 * j + 1]};
            t = (mm[4 * s + j] * c2 + t) * rr[4 * s + j];
            v[j] = pk2(t[0], t[1]);
          }
          st_ws(wsr, vb + voff[c5] + s * VT_TG, v);
        }
      }
    }
  }
#undef lane
#undef r
#undef hh
}

__global__ __launch_bounds__(512, 2) void k1_kernel(const char* __restrict__ x, const char* __restrict__ wqkv,
                                                    const char* __restrict__ wo, const char* __restrict__ wg,
                                                    const char* __restrict__ wu, const char* __restrict__ wd,
                                                    char* __restrict__ ws) {
  __shared__ __attribute__((aligned(16))) char smem[K1_LDS];
  LAS char* lds = (LAS char*)smem;
  const int b = blockIdx.x, xcd = b & 7, idx = b >> 3;
  const int rb = 8 * (xcd & 1) + (idx & 7), nt = 4 * (xcd >> 1) + (idx >> 3);
  if (b == 0 && threadIdx.x < 8) *(unsigned*)(ws + OFF_CNT + threadIdx.x * 256) = 0u;
  if (idx < 2 && __builtin_amdgcn_readfirstlane(threadIdx.x) < 64) warm_code<10>(threadIdx.x);
  if (nt < 12) k1_body<0>(x, wqkv, wg, wu, wd, wo, ws, lds, rb, nt);
  else if (nt < 14) k1_body<1>(x, wqkv, wg, wu, wd, wo, ws, lds, rb, nt);
  else k1_body<2>(x, wqkv, wg, wu, wd, wo, ws, lds, rb, nt);
}

#define PSLEEP 2
#define GSLEEP 8
constexpr int K2_K = 0;
constexpr int K2_Q = 24576;
constexpr int K2_V = 36864;
constexpr int K2_FLAG = K2_V + 21504;
constexpr int K2_RED = K2_FLAG + 64;
constexpr int RED_ROW = 336;
constexpr int K2_LDS = K2_RED + 4 * 64 * RED_ROW;

__global__ __launch_bounds__(768, 3) void k2_kernel(const char* __restrict__ x, char* __restrict__ ws,
                                                    char* __restrict__ out) {
  __shared__ __attribute__((aligned(16))) char smem[K2_LDS];
  LAS char* lds = (LAS char*)smem;
  const int tid = threadIdx.x, lane = tid & 63;
  const int w = __builtin_amdgcn_readfirstlane(tid >> 6);
  const int b = blockIdx.x, xcd = b & 7, idx = b >> 3;
  LAS unsigned* flg = (LAS unsigned*)(lds + K2_FLAG);
  if (tid < 8) flg[tid] = 0u;
  asm volatile("s_waitcnt lgkmcnt(0)\n\ts_barrier" ::: "memory");

  if (w < 8) {
    const int head = 4 * xcd + (idx & 3), rb = idx >> 2;
    const int r = lane & 31, hh = lane >> 5;
    {
      const unsigned l16 = lane * 16;
      const unsigned ldsb = (unsigned)(size_t)lds;
      auto dma = [&](const char* sbase, unsigned dst) {
        asm volatile("s_mov_b32 m0, %0\n\ts_nop 0\n\tglobal_load_lds_dwordx4 %1, %2" ::"s"(dst), "v"(l16), "s"(sbase) : "memory", "m0");
      };
      const char* qb_ = ws + OFF_KQ + (size_t)((head * 5) >> 1) * 32768 + rb * 4096 + (w & 3) * 1024;
      dma(qb_ + (w >> 2) * 32768, ldsb + K2_Q + w * 1024);
      dma(qb_ + 2 * 32768, ldsb + K2_Q + (8 + (w & 3)) * 1024);
      const char* kb_ = ws + OFF_KQ + (size_t)(80 + ((head * 5) >> 1)) * 32768 + rb * 4096 - 4096 + w * 1024;
      dma(kb_, ldsb + K2_K + w * 1024);
      dma(kb_ + 32768, ldsb + K2_K + (8 + w) * 1024);
      dma(kb_ + 65536, ldsb + K2_K + (16 + w) * 1024);
      const char* vb_ = ws + OFF_VT + (size_t)head * VT_HEAD + (16 * rb - 16) * VT_TG;
      const int j3 = (w + 16 <= 20) ? (w + 16) : w;
      dma(vb_ + w * 1024, ldsb + K2_V + w * 1024);
      dma(vb_ + (w + 8) * 1024, ldsb + K2_V + (w + 8) * 1024);
      dma(vb_ + j3 * 1024, ldsb + K2_V + j3 * 1024);
    }
    f32x16 mk0, mk8;
#pragma unroll
    for (int j = 0; j < 16; ++j) {
      const int kl = (j & 3) + 8 * (j >> 2) + 4 * hh;
      mk0[j] = (kl > r) ? 0.f : -INFINITY;
      mk8[j] = (kl <= r) ? 0.f : -INFINITY;
    }
    asm volatile("" : "+v"(mk0), "+v"(mk8));
    asm volatile("s_waitcnt vmcnt(3)" ::: "memory");
    if (lane == 0) __hip_atomic_fetch_add(flg, 1u, __ATOMIC_RELAXED, __HIP_MEMORY_SCOPE_WORKGROUP);
    for (int sp = 0; sp < (1 << 22); ++sp) {
      if (__hip_atomic_load(flg, __ATOMIC_RELAXED, __HIP_MEMORY_SCOPE_WORKGROUP) >= 8u) break;
      __builtin_amdgcn_s_sleep(1);
    }
    asm volatile("" ::: "memory");
    const int dcl = (r < 20) ? r : 20;
    const int qt = w;
    const int tmin = (rb == 0) ? (8 - qt) : 0;
    bf16x8 qa, qb;
    {
      LAS char* qp = lds + K2_Q + (32 * qt + r) * 16;
      qa = *(LAS bf16x8*)(qp + (hh + (head & 1)) * 4096);
      u32x2 c = *(LAS u32x2*)(qp + ((head & 1) ? 8 : 2 * 4096));
      u32x4 t1_ = {hh ? 0u : c[0], hh ? 0u : c[1], 0u, 0u};
      qb = __builtin_bit_cast(bf16x8, t1_);
    }
    LAS char* kp = lds + K2_K + (32 * qt + r) * 16;
    LAS char* kpa = kp + (hh + (head & 1)) * 8192;
    LAS char* kpc = kp + ((head & 1) ? 8 : 2 * 8192);
    bf16x8 ka[3], kc[3], vf[3][2];
    auto rdk = [&](int t) {
      ka[t % 3] = *(LAS bf16x8*)(kpa + t * 512);
      u32x2 c_ = *(LAS u32x2*)(kpc + t * 512);
      kc[t % 3] = __builtin_bit_cast(bf16x8, u32x4{c_[0], c_[1], 0u, 0u});
    };
    f32x16 sa, sb;
    float nm;
    {
      f32x16 z;
#pragma unroll
      for (int j = 0; j < 16; ++j) z[j] = 0.f;
      rdk(8); rdk(7);
      float mx[4];
      __builtin_amdgcn_sched_barrier(0);
#pragma unroll
      for (int t = 8; t >= -1; --t) {
        if (t >= 0) {
          f32x16 c0 = (t == 8) ? mk8 : ((t == 0) ? mk0 : z);
          f32x16 v = MFMA32(ka[t % 3], qa, c0);
          if (t - 2 >= 0) rdk(t - 2);
          v = MFMA32(kc[t % 3], qb, v);
          if (t & 1) sb = v; else sa = v;
        }
        if (t < 8) {
          const f32x16& p = ((t + 1) & 1) ? sb : sa;
          if (t + 1 == 8) {
#pragma unroll
            for (int u = 0; u < 4; ++u) mx[u] = fmaxf(fmaxf(p[4 * u], p[4 * u + 1]), fmaxf(p[4 * u + 2], p[4 * u + 3]));
          } else if (t + 1 >= tmin) {
#pragma unroll
            for (int j = 0; j < 16; j += 8)
#pragma unroll
              for (int u = 0; u < 4; ++u) mx[u] = fmaxf(fmaxf(mx[u], p[j + 2 * u]), p[j + 2 * u + 1]);
          }
          asm volatile("" : "+v"(mx[0]), "+v"(mx[1]), "+v"(mx[2]), "+v"(mx[3]));
        }
        __builtin_amdgcn_sched_barrier(0);
      }
      nm = -swap_max(fmaxf(fmaxf(mx[0], mx[1]), fmaxf(mx[2], mx[3])));
    }
    asm volatile("s_waitcnt vmcnt(0) lgkmcnt(0)" ::: "memory");
    {
      const int o1 = (lane >> 1) * VT_TG + (lane & 1) * 336 + 320;
      if (((o1 >> 10) & 7) == w) {
        u32x4 one = {0x3f803f80u, 0x3f803f80u, 0x3f803f80u, 0x3f803f80u};
        *(LAS u32x4*)(lds + K2_V + o1) = one;
      }
      asm volatile("s_waitcnt lgkmcnt(0)" ::: "memory");
    }
    if (lane == 0) __hip_atomic_fetch_add(flg + 2, 1u, __ATOMIC_RELAXED, __HIP_MEMORY_SCOPE_WORKGROUP);
    rdk(8); rdk(7);
    f32x16 cm;
#pragma unroll
    for (int j = 0; j < 16; ++j) { cm[j] = nm; mk0[j] += nm; mk8[j] += nm; }
    for (int sp = 0; sp < (1 << 22); ++sp) {
      if (__hip_atomic_load(flg + 2, __ATOMIC_RELAXED, __HIP_MEMORY_SCOPE_WORKGROUP) >= 8u) break;
      __builtin_amdgcn_s_sleep(1);
    }
    asm volatile("" ::: "memory");
    {
      LAS char* vp = lds + K2_V + (2 * qt) * VT_TG + dcl * 16 + hh * 336;
      auto rdv = [&](int t) {
        vf[t % 3][0] = *(LAS bf16x8*)(vp + (2 * t) * VT_TG);
        vf[t % 3][1] = *(LAS bf16x8*)(vp + (2 * t + 1) * VT_TG);
      };
      rdv(8);
      f32x16 o;
#pragma unroll
      for (int j = 0; j < 16; ++j) o[j] = 0.f;
      u32x4 pp[2];
      __builtin_amdgcn_sched_barrier(0);
#pragma unroll
      for (int t = 8; t >= -2; --t) {
        const bool ex = (t + 1 <= 8 && t + 1 >= 0);
        f32x16& p = ((t + 1) & 1) ? sb : sa;
        f32x16& v = (t & 1) ? sb : sa;
        auto exps = [&](int k) {
          if (ex) {
#pragma unroll
            for (int j = 4 * k; j < 4 * k + 4; ++j) p[j] = __builtin_amdgcn_exp2f(p[j]);
            asm volatile("" : "+v"(p));
          }
          __builtin_amdgcn_sched_barrier(0);
        };
        const bool pv = (t + 2 <= 8) && (t + 2 >= tmin);
        if (t + 2 <= 8) { if (pv) o = MFMA32(vf[(t + 2) % 3][0], __builtin_bit_cast(bf16x8, pp[0]), o); }
        exps(0);
        if (t + 2 <= 8) { if (pv) o = MFMA32(vf[(t + 2) % 3][1], __builtin_bit_cast(bf16x8, pp[1]), o); }
        if (t - 1 >= 0) rdv(t - 1);
        exps(1);
        if (t >= 0) {
          f32x16 c0 = (t == 8) ? mk8 : ((t == 0) ? mk0 : cm);
          v = MFMA32(ka[t % 3], qa, c0);
        }
        exps(2);
        if (t >= 0) {
          v = MFMA32(kc[t % 3], qb, v);
          if (t - 2 >= 0) rdk(t - 2);
        }
        exps(3);
        if (ex) {
#pragma unroll
          for (int s = 0; s < 2; ++s)
#pragma unroll
            for (int j = 0; j < 4; ++j) pp[s][j] = pk2(p[8 * s + 2 * j], p[8 * s + 2 * j + 1]);
          asm volatile("" : "+v"(pp[0]), "+v"(pp[1]));
        }
        __builtin_amdgcn_sched_barrier(0);
      }
      const float rl = __builtin_amdgcn_rcpf(o[12]);
      const int tok = 256 * rb + 32 * qt + r;
      char* ob = ws + OFF_OT + ((size_t)(tok >> 4) * 20 * 64 + (tok & 15)) * 16;
#pragma unroll
      for (int a = 0; a < 3; ++a) {
        const int k = head * 20 + 8 * a + 4 * hh;
        unsigned long long v = (unsigned long long)pk2(o[4 * a] * rl, o[4 * a + 1] * rl) |
                               ((unsigned long long)pk2(o[4 * a + 2] * rl, o[4 * a + 3] * rl) << 32);
        char* p = ob + (size_t)(k >> 5) * 1024 + ((k >> 3) & 3) * 256 + (k & 7) * 2;
        if (a < 2 || hh == 0)
          __hip_atomic_store((unsigned long long*)p, v, __ATOMIC_RELAXED, __HIP_MEMORY_SCOPE_AGENT);
      }
    }
    asm volatile("s_waitcnt vmcnt(0)" ::: "memory");
    if (lane == 0) {
      unsigned old = __hip_atomic_fetch_add(flg + 1, 1u, __ATOMIC_RELAXED, __HIP_MEMORY_SCOPE_WORKGROUP);
      if (old == 7u)
        __hip_atomic_fetch_add((unsigned*)(ws + OFF_CNT + rb * 256), 1u, __ATOMIC_RELAXED, __HIP_MEMORY_SCOPE_AGENT);
    }
    return;
  }

  const int kw = w - 8;
  const int rb64 = 8 * (xcd >> 1) + (idx & 7), ct = 4 * (xcd & 1) + (idx >> 3);
  const char* wp = ws + OFF_W2 + (size_t)ct * 68 * 5 * 1024 + lane * 16;
  const char* hp = ws + OFF_HT + (size_t)(4 * rb64) * 48 * 1024 + lane * 16;
  auto orsrc = __builtin_amdgcn_make_buffer_rsrc((void*)(ws + OFF_OT), 0, (int)SZ_OT, 0x00020000);
  const int ovoff = (4 * rb64) * 20 * 1024 + lane * 16;
  f32x4 acc[5][4];
#pragma unroll
  for (int n = 0; n < 5; ++n)
#pragma unroll
    for (int i = 0; i < 4; ++i) acc[n][i] = f32x4{0.f, 0.f, 0.f, 0.f};
  constexpr int GD = 2;
  bf16x8 bw[GD][5], bt[GD][4];
  auto loadT = [&](int st, int sl) {
    if (st < 12) {
      const int ks = 12 * kw + st;
#pragma unroll
      for (int i = 0; i < 4; ++i) bt[sl][i] = *(const bf16x8*)(hp + (size_t)(i * 48 + ks) * 1024);
    } else {
      const int ko = 5 * kw + (st - 12);
#pragma unroll
      for (int i = 0; i < 4; ++i)
        bt[sl][i] = __builtin_bit_cast(bf16x8, __builtin_amdgcn_raw_buffer_load_b128(orsrc, ovoff + (i * 20 + ko) * 1024, 0, 16));
    }
    __builtin_amdgcn_sched_barrier(0);
  };
  auto loadW = [&](int st, int sl) {
    const int ks = (st < 12) ? (12 * kw + st) : (48 + 5 * kw + (st - 12));
#pragma unroll
    for (int n = 0; n < 5; ++n) bw[sl][n] = *(const bf16x8*)(wp + (size_t)(ks * 5 + n) * 1024);
    __builtin_amdgcn_sched_barrier(0);
  };
  auto mm = [&](int st) {
#pragma unroll
    for (int n = 0; n < 5; ++n)
#pragma unroll
      for (int i = 0; i < 4; ++i) acc[n][i] = MFMA16(bw[st % GD][n], bt[st % GD][i], acc[n][i]);
    __builtin_amdgcn_sched_barrier(0);
  };
  __builtin_amdgcn_s_sleep(GSLEEP);
#pragma unroll
  for (int st = 0; st < GD; ++st) { loadT(st, st); loadW(st, st); }
#pragma unroll
  for (int st = 0; st < 12; ++st) {
    mm(st);
    if (st + GD < 12) loadT(st + GD, st % GD);
    loadW(st + GD, st % GD);
  }
  {
    if (kw == 0) {
      const unsigned* cnt = (const unsigned*)(ws + OFF_CNT + (rb64 >> 2) * 256);
      for (int sp = 0; sp < (1 << 22); ++sp) {
        if (__hip_atomic_load(cnt, __ATOMIC_RELAXED, __HIP_MEMORY_SCOPE_AGENT) >= 32u) break;
        __builtin_amdgcn_s_sleep(PSLEEP);
      }
      __hip_atomic_store(flg + 3, 1u, __ATOMIC_RELAXED, __HIP_MEMORY_SCOPE_WORKGROUP);
    } else {
      for (int sp = 0; sp < (1 << 24); ++sp) {
        if (__hip_atomic_load(flg + 3, __ATOMIC_RELAXED, __HIP_MEMORY_SCOPE_WORKGROUP) != 0u) break;
        __builtin_amdgcn_s_sleep(1);
      }
    }
    __builtin_amdgcn_fence(__ATOMIC_ACQUIRE, "wavefront");
    asm volatile("" ::: "memory");
  }
  loadT(12, 0); loadT(13, 1);
  mm(12); loadT(14, 0); loadW(14, 0);
  mm(13); loadT(15, 1); loadW(15, 1);
  mm(14); loadT(16, 0); loadW(16, 0);
  mm(15);
  mm(16);
  const int tq = tid - 512;
  u32x4 xv[3];
#pragma unroll
  for (int it = 0; it < 3; ++it) {
    const int item = tq + 256 * it;
    const int tk = item / 10, n8 = item - tk * 10;
    if (item < 640) xv[it] = *(const u32x4*)(x + (size_t)(64 * rb64 + tk) * 1280 + (80 * ct + 8 * n8) * 2);
  }
  {
    LAS char* rp = lds + K2_RED + (kw * 64 + (lane & 15)) * RED_ROW + (lane >> 4) * 16;
#pragma unroll
    for (int n = 0; n < 5; ++n)
#pragma unroll
      for (int i = 0; i < 4; ++i) *(LAS f32x4*)(rp + i * 16 * RED_ROW + n * 64) = acc[n][i];
  }
  asm volatile("s_waitcnt lgkmcnt(0)\n\ts_barrier" ::: "memory");
#pragma unroll
  for (int it = 0; it < 3; ++it) {
    const int item = tq + 256 * it;
    const int tk = item / 10, n8 = item - tk * 10;
    if (item < 640) {
      LAS char* rp = lds + K2_RED + tk * RED_ROW + n8 * 32;
      f32x4 a = *(LAS f32x4*)rp, c = *(LAS f32x4*)(rp + 16);
#pragma unroll
      for (int k = 1; k < 4; ++k) {
        a += *(LAS f32x4*)(rp + k * 64 * RED_ROW);
        c += *(LAS f32x4*)(rp + k * 64 * RED_ROW + 16);
      }
      u32x4 xx = xv[it];
      u32x4 v;
      v[0] = pk2(a[0] + __uint_as_float(xx[0] << 16), a[1] + __uint_as_float(xx[0] & 0xffff0000u));
      v[1] = pk2(a[2] + __uint_as_float(xx[1] << 16), a[3] + __uint_as_float(xx[1] & 0xffff0000u));
      v[2] = pk2(c[0] + __uint_as_float(xx[2] << 16), c[1] + __uint_as_float(xx[2] & 0xffff0000u));
      v[3] = pk2(c[2] + __uint_as_float(xx[3] << 16), c[3] + __uint_as_float(xx[3] & 0xffff0000u));
      *(u32x4*)(out + (size_t)(64 * rb64 + tk) * 1280 + (80 * ct + 8 * n8) * 2) = v;
    }
  }
}

}

extern "C" void kernel_launch(void* const* d_in, const int* in_sizes, int n_in, void* d_out, int out_size,
                              void* d_ws, size_t ws_size, hipStream_t stream) {
  const char* x = (const char*)d_in[0];
  const char* wqkv = (const char*)d_in[1];
  const char* wo = (const char*)d_in[2];
  const char* wg = (const char*)d_in[4];
  const char* wu = (const char*)d_in[5];
  const char* wd = (const char*)d_in[6];
  k1_kernel<<<256, 512, 0, stream>>>(x, wqkv, wo, wg, wu, wd, (char*)d_ws);
  k2_kernel<<<256, 768, 0, stream>>>(x, (char*)d_ws, (char*)d_out);
}
```

```cpp
#include <hip/hip_runtime.h>
#include <hip/hip_bf16.h>
#include <stdint.h>

typedef short bf16x8 __attribute__((ext_vector_type(8)));
typedef short s16x4 __attribute__((ext_vector_type(4)));
typedef float f32x16 __attribute__((ext_vector_type(16)));
typedef float f32x4 __attribute__((ext_vector_type(4)));
typedef float f32x2 __attribute__((ext_vector_type(2)));
typedef unsigned u32x4 __attribute__((ext_vector_type(4)));
typedef unsigned u32x2 __attribute__((ext_vector_type(2)));
typedef __bf16 bf2 __attribute__((ext_vector_type(2)));
#define LAS __attribute__((address_space(3)))
#define DI __device__ __forceinline__

namespace {

constexpr float QSCALE = 0.22360679774997896f * 1.4426950408889634f;

constexpr size_t OFF_CNT = 0;
constexpr size_t OFF_KQ = 32768;
constexpr size_t SZ_KQ = 160ull * 2048 * 16;
constexpr size_t OFF_VT = OFF_KQ + SZ_KQ;
constexpr int VT_TG = 21 * 32;
constexpr int VT_HEAD = 128 * VT_TG;
constexpr size_t SZ_VT = 32ull * VT_HEAD;
constexpr size_t OFF_HT = OFF_VT + SZ_VT;
constexpr size_t SZ_HT = 128ull * 48 * 1024;
constexpr size_t OFF_OT = OFF_HT + SZ_HT;
constexpr size_t SZ_OT = 128ull * 20 * 1024;
constexpr size_t OFF_W2 = OFF_OT + SZ_OT;

DI float swap_sum(float x) {
  auto r = __builtin_amdgcn_permlane32_swap(__float_as_uint(x), __float_as_uint(x), false, false);
  return __uint_as_float(r[0]) + __uint_as_float(r[1]);
}
DI float swap_max(float x) {
  auto r = __builtin_amdgcn_permlane32_swap(__float_as_uint(x), __float_as_uint(x), false, false);
  return fmaxf(__uint_as_float(r[0]), __uint_as_float(r[1]));
}
DI unsigned pk2(float a, float b) {
  f32x2 v = {a, b};
  return __builtin_bit_cast(unsigned, __builtin_convertvector(v, bf2));
}
DI s16x4 tr_read(LAS char* p) {
  return __builtin_amdgcn_ds_read_tr16_b64_v4i16((LAS s16x4*)p);
}
template <int N> DI void wait_bar() {
  asm volatile("s_waitcnt vmcnt(%0) lgkmcnt(0)\n\ts_barrier" ::"i"(N) : "memory");
}
#define MFMA32(a, b, c) __builtin_amdgcn_mfma_f32_32x32x16_bf16((a), (b), (c), 0, 0, 0)
#define MFMA16(a, b, c) __builtin_amdgcn_mfma_f32_16x16x32_bf16((a), (b), (c), 0, 0, 0)

constexpr int K1_WSLOT = 20480, K1_NWS = 5;
constexpr int K1_XOFF = K1_WSLOT * K1_NWS;
constexpr int K1_XSLOT = 16384, K1_NXS = 3;
constexpr int K1_CS = K1_XOFF + K1_XSLOT * K1_NXS;
constexpr int K1_ST = K1_CS + 2560;
constexpr int K1_LDS = K1_ST + 1024;

template <int NL, int STEP> DI void warm_code(int lane, unsigned& d) {
  const char* pc;
  asm volatile("s_getpc_b64 %0" : "=s"(pc));
  const unsigned off = lane * 128;
#pragma unroll
  for (int i = 0; i < NL; ++i) asm volatile("global_load_dword %0, %1, %2" : "+v"(d) : "v"(off), "s"(pc + i * STEP) : "memory");
}
DI float bf_lo(unsigned v) { return __uint_as_float(v << 16); }
DI float bf_hi(unsigned v) { return __uint_as_float(v & 0xffff0000u); }
DI void bar_only() { asm volatile("s_barrier" ::: "memory"); }

DI void st_ws(__amdgpu_buffer_rsrc_t rs, unsigned off, u32x4 v) { __builtin_amdgcn_raw_buffer_store_b128(v, rs, (int)off, 0, 0); }
DI void st_ws(__amdgpu_buffer_rsrc_t rs, unsigned off, u32x2 v) { __builtin_amdgcn_raw_buffer_store_b64(v, rs, (int)off, 0, 0); }

template <int TYPE>
DI void k1_body(const char* __restrict__ x, const char* __restrict__ wqkv, const char* __restrict__ wg,
                const char* __restrict__ wu, const char* __restrict__ wd, const char* __restrict__ wo,
                char* __restrict__ ws, LAS char* lds, int rb, int nt) {
  constexpr int NU = (TYPE == 1) ? 4 : 5;
  const int tid = threadIdx.x, lane = tid & 63;
  const int w = __builtin_amdgcn_readfirstlane(tid >> 6);
  const int qcol0 = (TYPE == 0) ? nt * 64 : (TYPE == 1) ? (12 + 4 * (nt - 12)) * 64 : 1280 + 5 * (nt - 14) * 64;

  if (w >= 4) {
    const int hw = w - 4;
    const char* ub[5];
    if (TYPE == 0) {
      ub[0] = wg + (2 * nt) * 128; ub[1] = wu + (2 * nt) * 128;
      ub[2] = wg + (2 * nt + 1) * 128; ub[3] = wu + (2 * nt + 1) * 128;
      ub[4] = wqkv + qcol0 * 2;
    } else {
      for (int u = 0; u < 5; ++u) ub[u] = wqkv + (qcol0 + 64 * u) * 2;
    }
    unsigned voffA, voffB, xvoff0, xvoff1;
    {
      int row = 8 * hw + (lane >> 3);
      int cc = ((((lane >> 2) & 1) ^ ((row >> 1) & 1)) << 2) + (lane & 3);
      voffA = row * 3072 + cc * 16;
      voffB = row * 3840 + cc * 16;
      int xr = rb * 128 + 32 * hw + (lane >> 3);
      int sc = (lane & 7) ^ (lane >> 4);
      xvoff0 = xr * 1280 + sc * 16;
      xvoff1 = xr * 1280 + (sc ^ 4) * 16;
    }
    const unsigned ldsb = (unsigned)(size_t)lds;
    auto dma = [&](const char* sbase, unsigned voff, unsigned dst) {
      asm volatile("s_mov_b32 m0, %0\n\ts_nop 0\n\tglobal_load_lds_dwordx4 %1, %2" ::"s"(dst), "v"(voff), "s"(sbase) : "memory", "m0");
    };
    const int coff = (blockIdx.x & 1) * 5 + ((blockIdx.x >> 1) & 3);
    auto issueW = [&](int slot) {
      const unsigned dst = ldsb + (slot % K1_NWS) * 4096 + hw * 1024;
      int gs = slot + 2 * coff; if (gs >= 20) gs -= 20;
#pragma unroll
      for (int u = 0; u < NU; ++u) {
        const bool a = (TYPE == 0 && u < 4);
        dma(ub[u] + (size_t)gs * 32 * (a ? 3072 : 3840), a ? voffA : voffB, dst + u * K1_WSLOT);
      }
    };
    auto issueX = [&](int c) {
      const unsigned dst = ldsb + K1_XOFF + (c % K1_NXS) * K1_XSLOT + hw * 4096;
      int gc = c + coff; if (gc >= 10) gc -= 10;
#pragma unroll
      for (int i = 0; i < 4; ++i) dma(x + gc * 128 + i * 8 * 1280, (i & 1) ? xvoff1 : xvoff0, dst + i * 1024);
    };
    issueW(0); issueX(0); issueW(1); issueW(2); issueX(1);
    wait_bar<2 * NU + 4>();
    issueW(3);
#pragma unroll
    for (int i = 0; i < 19; ++i) {
      const int nw = ((i + 3 < 19) ? (i + 3) : 19) - (i + 1);
      const bool hx = (i & 1) ? (i <= 15) : (i <= 16);
      const int cnt = NU * nw + (hx ? 4 : 0);
      if (cnt == 2 * NU + 4) wait_bar<2 * NU + 4>();
      else if (cnt == 2 * NU) wait_bar<2 * NU>();
      else if (cnt == NU) wait_bar<NU>();
      else wait_bar<0>();
      if (i + 4 < 20) issueW(i + 4);
      if ((i & 1) == 0 && (i / 2 + 2) < 10) issueX(i / 2 + 2);
    }
    bar_only();
    {
      const int xcd = blockIdx.x & 7, ks = (blockIdx.x >> 3) + 32 * hw;
      if (hw < 3 && ks < 68) {
        const char* src = (ks < 48) ? (wd + (size_t)(32 * ks) * 1280) : (wo + (size_t)(32 * (ks - 48)) * 1280);
        src += xcd * 160;
        const unsigned scr = hw * 5120;
#pragma unroll
        for (int j = 0; j < 5; ++j) {
          const int sl = j * 64 + lane, row = sl / 10, cc = sl - row * 10;
          dma(src, row * 1280 + cc * 16, ldsb + scr + j * 1024);
        }
        asm volatile("s_waitcnt vmcnt(0)" ::: "memory");
        LAS char* tp = lds + scr + (8 * (lane >> 4) + ((lane & 15) >> 2)) * 160 + 8 * (lane & 3);
        char* dst = ws + OFF_W2 + (size_t)((xcd * 68 + ks) * 5) * 1024 + lane * 16;
#pragma unroll
        for (int n = 0; n < 5; ++n) {
          s16x4 lo = tr_read(tp + n * 32), hi = tr_read(tp + n * 32 + 640);
          *(bf16x8*)(dst + n * 1024) = __builtin_shufflevector(lo, hi, 0, 1, 2, 3, 4, 5, 6, 7);
        }
      }
    }
    return;
  }

  const int tw = w & 1, ch = w >> 1;
  const int r = lane & 31, hh = lane >> 5;
  int xa[2][2];
  {
    int e = hh ^ ((r >> 1) & 7);
#pragma unroll
    for (int g = 0; g < 2; ++g)
#pragma unroll
      for (int ph = 0; ph < 2; ++ph) {
        int tg = 2 * tw + (g ^ ch), s = ph ^ tw;
        xa[g][ph] = K1_XOFF + (32 * tg + r) * 128 + (((2 * s) ^ e) << 4);
      }
  }
  int wa[2][3];
  {
    int G = lane >> 4, chh = G & 1, q = (lane & 15) >> 2, p = lane & 3;
#pragma unroll
    for (int ph = 0; ph < 2; ++ph) {
      int b0 = (8 * hh + q) * 128 + 32 * chh + 8 * p + (ph ^ tw) * 2048;
      wa[ph][0] = b0 + 64 * (0 ^ (q >> 1)) + ch * 2 * K1_WSLOT;
      wa[ph][1] = b0 + 64 * (1 ^ (q >> 1)) + ch * 2 * K1_WSLOT;
      wa[ph][2] = b0 + 64 * (ch ^ (q >> 1)) + 4 * K1_WSLOT;
    }
  }
  f32x16 acc[2][5];
#pragma unroll
  for (int g = 0; g < 2; ++g)
#pragma unroll
    for (int i = 0; i < 5; ++i)
#pragma unroll
      for (int j = 0; j < 16; ++j) acc[g][i][j] = 0.f;
  f32x4 cs[5];
#pragma unroll
  for (int i = 0; i < 5; ++i) cs[i] = f32x4{0.f, 0.f, 0.f, 0.f};
  bf16x8 onep;
  {
    const unsigned o = ((lane & 15) == ((lane >> 4) & 1)) ? 0x3f803f80u : 0u;
    u32x4 t = {o, o, o, o};
    onep = __builtin_bit_cast(bf16x8, t);
  }
  float s0 = 0.f, q0 = 0.f;

  bf16x8 xf[2];
  s16x4 wl[5], wh[5];
  auto rdx = [&](int i, int ph, int g) {
    LAS char* xb = lds + ((i / 2) % K1_NXS) * K1_XSLOT;
    xf[g] = *(LAS bf16x8*)(xb + (xa[g][ph] ^ ((i & 1) * 64)));
  };
  auto rdw = [&](int i, int ph, int c5) {
    LAS char* p = lds + (i % K1_NWS) * 4096 + ((c5 < 4) ? ((c5 >> 1) * K1_WSLOT + wa[ph][c5 & 1]) : wa[ph][2]);
    wl[c5] = tr_read(p);
    wh[c5] = tr_read(p + 512);
  };
  auto phase = [&](int i, int ph, int ni, int nph, bool do_rd) {
#pragma unroll
    for (int k = 0; k < 2 * NU; ++k) {
      const int g = (k >= NU) ? 1 : 0, c5 = k - g * NU;
      bf16x8 wfr = __builtin_shufflevector(wl[c5], wh[c5], 0, 1, 2, 3, 4, 5, 6, 7);
      if (TYPE == 2) acc[g][c5] = MFMA32(xf[g], wfr, acc[g][c5]);
      else acc[g][c5] = MFMA32(wfr, xf[g], acc[g][c5]);
      if (ph == 0 && g == 0) cs[c5] = MFMA16(wfr, onep, cs[c5]);
      if (k == 0) rdx(i, ph, 1);
      if (do_rd && g == 1) {
        if (c5 == 0) rdx(ni, nph, 0);
        rdw(ni, nph, c5);
      }
      if (k < 4) {
        u32x4 xu = __builtin_bit_cast(u32x4, xf[0]);
        float l = bf_lo(xu[k]), h = bf_hi(xu[k]);
        s0 += l; q0 = fmaf(l, l, q0); s0 += h; q0 = fmaf(h, h, q0);
        asm volatile("" : "+v"(s0), "+v"(q0));
      }
      __builtin_amdgcn_sched_barrier(0);
    }
  };

  bar_only();
  rdx(0, 0, 0);
#pragma unroll
  for (int c5 = 0; c5 < NU; ++c5) rdw(0, 0, c5);
  __builtin_amdgcn_sched_barrier(0);
#pragma unroll
  for (int i = 0; i < 20; ++i) {
    phase(i, 0, i, 1, true);
    if (i < 19) {
      bar_only();
      __builtin_amdgcn_sched_barrier(0);
      phase(i, 1, i + 1, 0, true);
    } else {
      phase(i, 1, 0, 0, false);
    }
  }

  int tz = threadIdx.x;
  asm volatile("" : "+v"(tz));
  const int lane_ = tz & 63, r_ = tz & 31, hh_ = (tz >> 5) & 1;
#define lane lane_
#define r r_
#define hh hh_
  LAS float* csb = (LAS float*)(lds + K1_CS) + ch * 160;
  const __amdgpu_buffer_rsrc_t wsr = __builtin_amdgcn_make_buffer_rsrc(ws, 0, 0x7fffffff, 0x00020000);
  {
    const float ssum = swap_sum(s0), ssq = swap_sum(q0);
    const float mean = ssum * (1.f / 640.f);
    const float var = fmaxf(ssq * (1.f / 640.f) - mean * mean, 0.f);
    const float rs = __builtin_amdgcn_rsqf(var + 1e-5f);
    if (lane < 32) {
      f32x2 st = {-mean, rs};
      *(LAS f32x2*)(lds + K1_ST + (32 * (2 * tw + ch) + lane) * 8) = st;
    }
    if ((lane & 15) < 2) {
      LAS float* cp = csb + tw * 320 + 16 * (lane & 15) + 4 * (lane >> 4);
#pragma unroll
      for (int c5 = 0; c5 < NU; ++c5) *(LAS f32x4*)(cp + c5 * 32) = cs[c5];
    }
  }
  asm volatile("s_waitcnt lgkmcnt(0)\n\ts_barrier" ::: "memory");

  if (TYPE != 2) {
    f32x2 st[2];
#pragma unroll
    for (int g = 0; g < 2; ++g) st[g] = *(LAS f32x2*)(lds + K1_ST + (32 * (2 * tw + (g ^ ch)) + r) * 8);
#pragma unroll
    for (int c5 = 0; c5 < NU; ++c5) {
      f32x4 c4[4];
#pragma unroll
      for (int q4 = 0; q4 < 4; ++q4) c4[q4] = *(LAS f32x4*)(csb + c5 * 32 + 8 * q4 + 4 * hh) + *(LAS f32x4*)(csb + 320 + c5 * 32 + 8 * q4 + 4 * hh);
#pragma unroll
      for (int g = 0; g < 2; ++g) {
        const f32x2 nm2 = {st[g][0], st[g][0]};
#pragma unroll
        for (int q4 = 0; q4 < 4; ++q4)
#pragma unroll
          for (int j = 0; j < 2; ++j) {
            f32x2 v = {acc[g][c5][4 * q4 + 2 * j], acc[g][c5][4 * q4 + 2 * j + 1]};
            v = nm2 * f32x2{c4[q4][2 * j], c4[q4][2 * j + 1]} + v;
            acc[g][c5][4 * q4 + 2 * j] = v[0]; acc[g][c5][4 * q4 + 2 * j + 1] = v[1];
          }
      }
    }
#pragma unroll
    for (int g = 0; g < 2; ++g) {
      const int tok = rb * 128 + 32 * (2 * tw + (g ^ ch)) + r;
      const float rs = st[g][1];
      auto store_qk = [&](const f32x16& a, int nb) {
        const unsigned kq = (unsigned)OFF_KQ + tok * 16 + (nb / 8 + hh) * 32768;
        const float rsq = (nb < 640) ? rs * QSCALE : rs;
        const f32x2 rs2 = {rsq, rsq};
        unsigned P[8];
#pragma unroll
        for (int j = 0; j < 8; ++j) {
          f32x2 v = f32x2{a[2 * j], a[2 * j + 1]} * rs2;
          P[j] = pk2(v[0], v[1]);
        }
#pragma unroll
        for (int qp = 0; qp < 2; ++qp) {
          auto r0 = __builtin_amdgcn_permlane32_swap(P[4 * qp], P[4 * qp + 2], false, false);
          auto r1 = __builtin_amdgcn_permlane32_swap(P[4 * qp + 1], P[4 * qp + 3], false, false);
          u32x4 v = {r0[0], r1[0], r0[1], r1[1]};
          st_ws(wsr, kq + qp * 65536, v);
        }
      };
      if (TYPE == 0) {
        store_qk(acc[g][4], qcol0 + 32 * ch);
        const unsigned ht = (unsigned)OFF_HT + ((tok >> 4) * 48 * 64 + (tok & 15) + 16 * hh) * 16 + (2 * (2 * nt + ch)) * 1024;
        const float k1 = -1.4426950408889634f * rs;
        const f32x2 k12 = {k1, k1};
        const float irs2 = __builtin_amdgcn_rcpf(rs * rs);
        const f32x2 ir2 = {irs2, irs2};
#pragma unroll
        for (int c = 0; c < 2; ++c) {
          unsigned P[8];
#pragma unroll
          for (int j = 0; j < 8; ++j) {
            f32x2 gg = {acc[g][c][2 * j], acc[g][c][2 * j + 1]};
            f32x2 uu = {acc[g][2 + c][2 * j], acc[g][2 + c][2 * j + 1]};
            f32x2 t = gg * k12;
            f32x2 e = {__builtin_amdgcn_exp2f(t[0]), __builtin_amdgcn_exp2f(t[1])};
            f32x2 d = e * ir2 + ir2;
            f32x2 rr = {__builtin_amdgcn_rcpf(d[0]), __builtin_amdgcn_rcpf(d[1])};
            f32x2 hv = (gg * uu) * rr;
            P[j] = pk2(hv[0], hv[1]);
          }
#pragma unroll
          for (int qp = 0; qp < 2; ++qp) {
            auto r0 = __builtin_amdgcn_permlane32_swap(P[4 * qp], P[4 * qp + 2], false, false);
            auto r1 = __builtin_amdgcn_permlane32_swap(P[4 * qp + 1], P[4 * qp + 3], false, false);
            u32x4 v = {r0[0], r1[0], r0[1], r1[1]};
            st_ws(wsr, ht + c * 1024 + qp * 512, v);
          }
        }
      } else {
#pragma unroll
        for (int c5 = 0; c5 < 4; ++c5) store_qk(acc[g][c5], qcol0 + 128 * ch + 32 * c5);
      }
    }
  } else {
    int voff[5];
    float cn[5];
#pragma unroll
    for (int c5 = 0; c5 < 5; ++c5) {
      const int nv = qcol0 - 1280 + ((c5 < 4) ? (128 * ch + 32 * c5) : (256 + 32 * ch)) + r;
      const int head = nv / 20, d = nv - head * 20;
      voff[c5] = head * VT_HEAD + hh * 336 + d * 16;
      cn[c5] = csb[c5 * 32 + r] + csb[320 + c5 * 32 + r];
    }
#pragma unroll
    for (int g = 0; g < 2; ++g) {
      const int tb = 32 * (2 * tw + (g ^ ch));
      f32x2 mm[8], rr[8];
#pragma unroll
      for (int q4 = 0; q4 < 4; ++q4) {
        f32x4 a = *(LAS f32x4*)(lds + K1_ST + (tb + 8 * q4 + 4 * hh) * 8);
        f32x4 b = *(LAS f32x4*)(lds + K1_ST + (tb + 8 * q4 + 4 * hh) * 8 + 16);
        mm[2 * q4] = f32x2{a[0], a[2]}; rr[2 * q4] = f32x2{a[1], a[3]};
        mm[2 * q4 + 1] = f32x2{b[0], b[2]}; rr[2 * q4 + 1] = f32x2{b[1], b[3]};
      }
      const unsigned vb = (unsigned)OFF_VT + ((rb * 128 + tb) >> 4) * VT_TG;
#pragma unroll
      for (int c5 = 0; c5 < 5; ++c5) {
        const f32x2 c2 = {cn[c5], cn[c5]};
#pragma unroll
        for (int s = 0; s < 2; ++s) {
          u32x4 v;
#pragma unroll
          for (int j = 0; j < 4; ++j) {
            f32x2 t = {acc[g][c5][8 * s + 2 * j], acc[g][c5][8 * s + 2 * j + 1]};
            t = (mm[4 * s + j] * c2 + t) * rr[4 * s + j];
            v[j] = pk2(t[0], t[1]);
          }
          st_ws(wsr, vb + voff[c5] + s * VT_TG, v);
        }
      }
    }
  }
#undef lane
#undef r
#undef hh
}

__global__ __launch_bounds__(512, 2) void k1_kernel(const char* __restrict__ x, const char* __restrict__ wqkv,
                                                    const char* __restrict__ wo, const char* __restrict__ wg,
                                                    const char* __restrict__ wu, const char* __restrict__ wd,
                                                    char* __restrict__ ws) {
  __shared__ __attribute__((aligned(16))) char smem[K1_LDS];
  LAS char* lds = (LAS char*)smem;
  const int b = blockIdx.x, xcd = b & 7, idx = b >> 3;
  const int rb = 8 * (xcd & 1) + (idx & 7), nt = 4 * (xcd >> 1) + (idx >> 3);
  if (b == 0 && threadIdx.x < 8) *(unsigned*)(ws + OFF_CNT + threadIdx.x * 256) = 0u;
  if (idx < 2 && __builtin_amdgcn_readfirstlane(threadIdx.x) < 64) {
    unsigned d = 0;
    warm_code<8, 8000>(threadIdx.x, d);
    asm volatile("s_waitcnt vmcnt(0)" : "+v"(d) :: "memory");
  }
  if (nt < 12) k1_body<0>(x, wqkv, wg, wu, wd, wo, ws, lds, rb, nt);
  else if (nt < 14) k1_body<1>(x, wqkv, wg, wu, wd, wo, ws, lds, rb, nt);
  else k1_body<2>(x, wqkv, wg, wu, wd, wo, ws, lds, rb, nt);
}

#define PSLEEP 2
#define GSLEEP 8
constexpr int K2_K = 0;
constexpr int K2_Q = 24576;
constexpr int K2_V = 36864;
constexpr int K2_FLAG = K2_V + 21504;
constexpr int K2_RED = K2_FLAG + 64;
constexpr int RED_ROW = 336;
constexpr int K2_LDS = K2_RED + 4 * 64 * RED_ROW;

__global__ __launch_bounds__(768, 3) void k2_kernel(const char* __restrict__ x, char* __restrict__ ws,
                                                    char* __restrict__ out) {
  __shared__ __attribute__((aligned(16))) char smem[K2_LDS];
  LAS char* lds = (LAS char*)smem;
  const int tid = threadIdx.x, lane = tid & 63;
  const int w = __builtin_amdgcn_readfirstlane(tid >> 6);
  const int b = blockIdx.x, xcd = b & 7, idx = b >> 3;
  LAS unsigned* flg = (LAS unsigned*)(lds + K2_FLAG);
  if (tid < 8) flg[tid] = 0u;
  unsigned wcd = 0;
  if (idx < 2 && w == 11) warm_code<2, 5600>(lane, wcd);
  asm volatile("s_waitcnt lgkmcnt(0)\n\ts_barrier" ::: "memory");

  if (w < 8) {
    const int head = 4 * xcd + (idx & 3), rb = idx >> 2;
    const int r = lane & 31, hh = lane >> 5;
    {
      const unsigned l16 = lane * 16;
      const unsigned ldsb = (unsigned)(size_t)lds;
      auto dma = [&](const char* sbase, unsigned dst) {
        asm volatile("s_mov_b32 m0, %0\n\ts_nop 0\n\tglobal_load_lds_dwordx4 %1, %2" ::"s"(dst), "v"(l16), "s"(sbase) : "memory", "m0");
      };
      const char* qb_ = ws + OFF_KQ + (size_t)((head * 5) >> 1) * 32768 + rb * 4096 + (w & 3) * 1024;
      dma(qb_ + (w >> 2) * 32768, ldsb + K2_Q + w * 1024);
      dma(qb_ + 2 * 32768, ldsb + K2_Q + (8 + (w & 3)) * 1024);
      const char* kb_ = ws + OFF_KQ + (size_t)(80 + ((head * 5) >> 1)) * 32768 + rb * 4096 - 4096 + w * 1024;
      dma(kb_, ldsb + K2_K + w * 1024);
      dma(kb_ + 32768, ldsb + K2_K + (8 + w) * 1024);
      dma(kb_ + 65536, ldsb + K2_K + (16 + w) * 1024);
      const char* vb_ = ws + OFF_VT + (size_t)head * VT_HEAD + (16 * rb - 16) * VT_TG;
      const int j3 = (w + 16 <= 20) ? (w + 16) : w;
      dma(vb_ + w * 1024, ldsb + K2_V + w * 1024);
      dma(vb_ + (w + 8) * 1024, ldsb + K2_V + (w + 8) * 1024);
      dma(vb_ + j3 * 1024, ldsb + K2_V + j3 * 1024);
    }
    f32x16 mk0, mk8;
#pragma unroll
    for (int j = 0; j < 16; ++j) {
      const int kl = (j & 3) + 8 * (j >> 2) + 4 * hh;
      mk0[j] = (kl > r) ? 0.f : -INFINITY;
      mk8[j] = (kl <= r) ? 0.f : -INFINITY;
    }
    asm volatile("" : "+v"(mk0), "+v"(mk8));
    asm volatile("s_waitcnt vmcnt(3)" ::: "memory");
    if (lane == 0) __hip_atomic_fetch_add(flg, 1u, __ATOMIC_RELAXED, __HIP_MEMORY_SCOPE_WORKGROUP);
    for (int sp = 0; sp < (1 << 22); ++sp) {
      if (__hip_atomic_load(flg, __ATOMIC_RELAXED, __HIP_MEMORY_SCOPE_WORKGROUP) >= 8u) break;
      __builtin_amdgcn_s_sleep(1);
    }
    asm volatile("" ::: "memory");
    const int dcl = (r < 20) ? r : 20;
    const int qt = w;
    const int tmin = (rb == 0) ? (8 - qt) : 0;
    bf16x8 qa, qb;
    {
      LAS char* qp = lds + K2_Q + (32 * qt + r) * 16;
      qa = *(LAS bf16x8*)(qp + (hh + (head & 1)) * 4096);
      u32x2 c = *(LAS u32x2*)(qp + ((head & 1) ? 8 : 2 * 4096));
      u32x4 t1_ = {hh ? 0u : c[0], hh ? 0u : c[1], 0u, 0u};
      qb = __builtin_bit_cast(bf16x8, t1_);
    }
    LAS char* kp = lds + K2_K + (32 * qt + r) * 16;
    LAS char* kpa = kp + (hh + (head & 1)) * 8192;
    LAS char* kpc = kp + ((head & 1) ? 8 : 2 * 8192);
    bf16x8 ka[3], kc[3], vf[3][2];
    auto rdk = [&](int t) {
      ka[t % 3] = *(LAS bf16x8*)(kpa + t * 512);
      u32x2 c_ = *(LAS u32x2*)(kpc + t * 512);
      kc[t % 3] = __builtin_bit_cast(bf16x8, u32x4{c_[0], c_[1], 0u, 0u});
    };
    f32x16 sa, sb;
    float nm;
    {
      f32x16 z;
#pragma unroll
      for (int j = 0; j < 16; ++j) z[j] = 0.f;
      rdk(8); rdk(7);
      float mx[4];
      __builtin_amdgcn_sched_barrier(0);
#pragma unroll
      for (int t = 8; t >= -1; --t) {
        if (t >= 0) {
          f32x16 c0 = (t == 8) ? mk8 : ((t == 0) ? mk0 : z);
          f32x16 v = MFMA32(ka[t % 3], qa, c0);
          if (t - 2 >= 0) rdk(t - 2);
          v = MFMA32(kc[t % 3], qb, v);
          if (t & 1) sb = v; else sa = v;
        }
        if (t < 8) {
          const f32x16& p = ((t + 1) & 1) ? sb : sa;
          if (t + 1 == 8) {
#pragma unroll
            for (int u = 0; u < 4; ++u) mx[u] = fmaxf(fmaxf(p[4 * u], p[4 * u + 1]), fmaxf(p[4 * u + 2], p[4 * u + 3]));
          } else if (t + 1 >= tmin) {
#pragma unroll
            for (int j = 0; j < 16; j += 8)
#pragma unroll
              for (int u = 0; u < 4; ++u) mx[u] = fmaxf(fmaxf(mx[u], p[j + 2 * u]), p[j + 2 * u + 1]);
          }
          asm volatile("" : "+v"(mx[0]), "+v"(mx[1]), "+v"(mx[2]), "+v"(mx[3]));
        }
        __builtin_amdgcn_sched_barrier(0);
      }
      nm = -swap_max(fmaxf(fmaxf(mx[0], mx[1]), fmaxf(mx[2], mx[3])));
    }
    asm volatile("s_waitcnt vmcnt(0) lgkmcnt(0)" ::: "memory");
    {
      const int o1 = (lane >> 1) * VT_TG + (lane & 1) * 336 + 320;
      if (((o1 >> 10) & 7) == w) {
        u32x4 one = {0x3f803f80u, 0x3f803f80u, 0x3f803f80u, 0x3f803f80u};
        *(LAS u32x4*)(lds + K2_V + o1) = one;
      }
      asm volatile("s_waitcnt lgkmcnt(0)" ::: "memory");
    }
    if (lane == 0) __hip_atomic_fetch_add(flg + 2, 1u, __ATOMIC_RELAXED, __HIP_MEMORY_SCOPE_WORKGROUP);
    rdk(8); rdk(7);
    f32x16 cm;
#pragma unroll
    for (int j = 0; j < 16; ++j) { cm[j] = nm; mk0[j] += nm; mk8[j] += nm; }
    for (int sp = 0; sp < (1 << 22); ++sp) {
      if (__hip_atomic_load(flg + 2, __ATOMIC_RELAXED, __HIP_MEMORY_SCOPE_WORKGROUP) >= 8u) break;
      __builtin_amdgcn_s_sleep(1);
    }
    asm volatile("" ::: "memory");
    {
      LAS char* vp = lds + K2_V + (2 * qt) * VT_TG + dcl * 16 + hh * 336;
      auto rdv = [&](int t) {
        vf[t % 3][0] = *(LAS bf16x8*)(vp + (2 * t) * VT_TG);
        vf[t % 3][1] = *(LAS bf16x8*)(vp + (2 * t + 1) * VT_TG);
      };
      rdv(8);
      f32x16 o;
#pragma unroll
      for (int j = 0; j < 16; ++j) o[j] = 0.f;
      u32x4 pp[2];
      __builtin_amdgcn_sched_barrier(0);
#pragma unroll
      for (int t = 8; t >= -2; --t) {
        const bool ex = (t + 1 <= 8 && t + 1 >= 0);
        f32x16& p = ((t + 1) & 1) ? sb : sa;
        f32x16& v = (t & 1) ? sb : sa;
        auto exps = [&](int k) {
          if (ex) {
#pragma unroll
            for (int j = 4 * k; j < 4 * k + 4; ++j) p[j] = __builtin_amdgcn_exp2f(p[j]);
            asm volatile("" : "+v"(p));
          }
          __builtin_amdgcn_sched_barrier(0);
        };
        const bool pv = (t + 2 <= 8) && (t + 2 >= tmin);
        if (t + 2 <= 8) { if (pv) o = MFMA32(vf[(t + 2) % 3][0], __builtin_bit_cast(bf16x8, pp[0]), o); }
        exps(0);
        if (t + 2 <= 8) { if (pv) o = MFMA32(vf[(t + 2) % 3][1], __builtin_bit_cast(bf16x8, pp[1]), o); }
        if (t - 1 >= 0) rdv(t - 1);
        exps(1);
        if (t >= 0) {
          f32x16 c0 = (t == 8) ? mk8 : ((t == 0) ? mk0 : cm);
          v = MFMA32(ka[t % 3], qa, c0);
        }
        exps(2);
        if (t >= 0) {
          v = MFMA32(kc[t % 3], qb, v);
          if (t - 2 >= 0) rdk(t - 2);
        }
        exps(3);
        if (ex) {
#pragma unroll
          for (int s = 0; s < 2; ++s)
#pragma unroll
            for (int j = 0; j < 4; ++j) pp[s][j] = pk2(p[8 * s + 2 * j], p[8 * s + 2 * j + 1]);
          asm volatile("" : "+v"(pp[0]), "+v"(pp[1]));
        }
        __builtin_amdgcn_sched_barrier(0);
      }
      const float rl = __builtin_amdgcn_rcpf(o[12]);
      const int tok = 256 * rb + 32 * qt + r;
      char* ob = ws + OFF_OT + ((size_t)(tok >> 4) * 20 * 64 + (tok & 15)) * 16;
#pragma unroll
      for (int a = 0; a < 3; ++a) {
        const int k = head * 20 + 8 * a + 4 * hh;
        unsigned long long v = (unsigned long long)pk2(o[4 * a] * rl, o[4 * a + 1] * rl) |
                               ((unsigned long long)pk2(o[4 * a + 2] * rl, o[4 * a + 3] * rl) << 32);
        char* p = ob + (size_t)(k >> 5) * 1024 + ((k >> 3) & 3) * 256 + (k & 7) * 2;
        if (a < 2 || hh == 0)
          __hip_atomic_store((unsigned long long*)p, v, __ATOMIC_RELAXED, __HIP_MEMORY_SCOPE_AGENT);
      }
    }
    asm volatile("s_waitcnt vmcnt(0)" ::: "memory");
    if (lane == 0) {
      unsigned old = __hip_atomic_fetch_add(flg + 1, 1u, __ATOMIC_RELAXED, __HIP_MEMORY_SCOPE_WORKGROUP);
      if (old == 7u)
        __hip_atomic_fetch_add((unsigned*)(ws + OFF_CNT + rb * 256), 1u, __ATOMIC_RELAXED, __HIP_MEMORY_SCOPE_AGENT);
    }
    return;
  }

  const int kw = w - 8;
  const int rb64 = 8 * (xcd >> 1) + (idx & 7), ct = 4 * (xcd & 1) + (idx >> 3);
  const char* wp = ws + OFF_W2 + (size_t)ct * 68 * 5 * 1024 + lane * 16;
  const char* hp = ws + OFF_HT + (size_t)(4 * rb64) * 48 * 1024 + lane * 16;
  auto orsrc = __builtin_amdgcn_make_buffer_rsrc((void*)(ws + OFF_OT), 0, (int)SZ_OT, 0x00020000);
  const int ovoff = (4 * rb64) * 20 * 1024 + lane * 16;
  f32x4 acc[5][4];
#pragma unroll
  for (int n = 0; n < 5; ++n)
#pragma unroll
    for (int i = 0; i < 4; ++i) acc[n][i] = f32x4{0.f, 0.f, 0.f, 0.f};
  constexpr int GD = 2;
  bf16x8 bw[GD][5], bt[GD][4];
  auto loadT = [&](int st, int sl) {
    if (st < 12) {
      const int ks = 12 * kw + st;
#pragma unroll
      for (int i = 0; i < 4; ++i) bt[sl][i] = *(const bf16x8*)(hp + (size_t)(i * 48 + ks) * 1024);
    } else {
      const int ko = 5 * kw + (st - 12);
#pragma unroll
      for (int i = 0; i < 4; ++i)
        bt[sl][i] = __builtin_bit_cast(bf16x8, __builtin_amdgcn_raw_buffer_load_b128(orsrc, ovoff + (i * 20 + ko) * 1024, 0, 16));
    }
    __builtin_amdgcn_sched_barrier(0);
  };
  auto loadW = [&](int st, int sl) {
    const int ks = (st < 12) ? (12 * kw + st) : (48 + 5 * kw + (st - 12));
#pragma unroll
    for (int n = 0; n < 5; ++n) bw[sl][n] = *(const bf16x8*)(wp + (size_t)(ks * 5 + n) * 1024);
    __builtin_amdgcn_sched_barrier(0);
  };
  auto mm = [&](int st) {
#pragma unroll
    for (int n = 0; n < 5; ++n)
#pragma unroll
      for (int i = 0; i < 4; ++i) acc[n][i] = MFMA16(bw[st % GD][n], bt[st % GD][i], acc[n][i]);
    __builtin_amdgcn_sched_barrier(0);
  };
  __builtin_amdgcn_s_sleep(GSLEEP);
#pragma unroll
  for (int st = 0; st < GD; ++st) { loadT(st, st); loadW(st, st); }
#pragma unroll
  for (int st = 0; st < 12; ++st) {
    mm(st);
    if (st == 0) asm volatile("" :: "v"(wcd));
    if (st + GD < 12) loadT(st + GD, st % GD);
    loadW(st + GD, st % GD);
  }
  {
    if (kw == 0) {
      const unsigned* cnt = (const unsigned*)(ws + OFF_CNT + (rb64 >> 2) * 256);
      for (int sp = 0; sp < (1 << 22); ++sp) {
        if (__hip_atomic_load(cnt, __ATOMIC_RELAXED, __HIP_MEMORY_SCOPE_AGENT) >= 32u) break;
        __builtin_amdgcn_s_sleep(PSLEEP);
      }
      __hip_atomic_store(flg + 3, 1u, __ATOMIC_RELAXED, __HIP_MEMORY_SCOPE_WORKGROUP);
    } else {
      for (int sp = 0; sp < (1 << 24); ++sp) {
        if (__hip_atomic_load(flg + 3, __ATOMIC_RELAXED, __HIP_MEMORY_SCOPE_WORKGROUP) != 0u) break;
        __builtin_amdgcn_s_sleep(1);
      }
    }
    __builtin_amdgcn_fence(__ATOMIC_ACQUIRE, "wavefront");
    asm volatile("" ::: "memory");
  }
  loadT(12, 0); loadT(13, 1);
  mm(12); loadT(14, 0); loadW(14, 0);
  mm(13); loadT(15, 1); loadW(15, 1);
  mm(14); loadT(16, 0); loadW(16, 0);
  mm(15);
  mm(16);
  const int tq = tid - 512;
  u32x4 xv[3];
#pragma unroll
  for (int it = 0; it < 3; ++it) {
    const int item = tq + 256 * it;
    const int tk = item / 10, n8 = item - tk * 10;
    if (item < 640) xv[it] = *(const u32x4*)(x + (size_t)(64 * rb64 + tk) * 1280 + (80 * ct + 8 * n8) * 2);
  }
  {
    LAS char* rp = lds + K2_RED + (kw * 64 + (lane & 15)) * RED_ROW + (lane >> 4) * 16;
#pragma unroll
    for (int n = 0; n < 5; ++n)
#pragma unroll
      for (int i = 0; i < 4; ++i) *(LAS f32x4*)(rp + i * 16 * RED_ROW + n * 64) = acc[n][i];
  }
  asm volatile("s_waitcnt lgkmcnt(0)\n\ts_barrier" ::: "memory");
#pragma unroll
  for (int it = 0; it < 3; ++it) {
    const int item = tq + 256 * it;
    const int tk = item / 10, n8 = item - tk * 10;
    if (item < 640) {
      LAS char* rp = lds + K2_RED + tk * RED_ROW + n8 * 32;
      f32x4 a = *(LAS f32x4*)rp, c = *(LAS f32x4*)(rp + 16);
#pragma unroll
      for (int k = 1; k < 4; ++k) {
        a += *(LAS f32x4*)(rp + k * 64 * RED_ROW);
        c += *(LAS f32x4*)(rp + k * 64 * RED_ROW + 16);
      }
      u32x4 xx = xv[it];
      u32x4 v;
      v[0] = pk2(a[0] + __uint_as_float(xx[0] << 16), a[1] + __uint_as_float(xx[0] & 0xffff0000u));
      v[1] = pk2(a[2] + __uint_as_float(xx[1] << 16), a[3] + __uint_as_float(xx[1] & 0xffff0000u));
      v[2] = pk2(c[0] + __uint_as_float(xx[2] << 16), c[1] + __uint_as_float(xx[2] & 0xffff0000u));
      v[3] = pk2(c[2] + __uint_as_float(xx[3] << 16), c[3] + __uint_as_float(xx[3] & 0xffff0000u));
      *(u32x4*)(out + (size_t)(64 * rb64 + tk) * 1280 + (80 * ct + 8 * n8) * 2) = v;
    }
  }
}

}

extern "C" void kernel_launch(void* const* d_in, const int* in_sizes, int n_in, void* d_out, int out_size,
                              void* d_ws, size_t ws_size, hipStream_t stream) {
  const char* x = (const char*)d_in[0];
  const char* wqkv = (const char*)d_in[1];
  const char* wo = (const char*)d_in[2];
  const char* wg = (const char*)d_in[4];
  const char* wu = (const char*)d_in[5];
  const char* wd = (const char*)d_in[6];
  k1_kernel<<<256, 512, 0, stream>>>(x, wqkv, wo, wg, wu, wd, (char*)d_ws);
  k2_kernel<<<256, 768, 0, stream>>>(x, (char*)d_ws, (char*)d_out);
}
```
